```python
import torch

def _softmax(x, axis=-1):
    return torch.softmax(x, dim=axis)
def _gelu(x):
    return torch.nn.functional.gelu(x, approximate="tanh")
def _silu(x):
    return torch.nn.functional.silu(x)
def _sigmoid(x):
    return torch.sigmoid(x)
def _layernorm(x, axis=-1, eps=1e-5):
    m = x.mean(dim=axis, keepdim=True)
    v = x.var(dim=axis, keepdim=True, unbiased=False)
    return (x - m) / torch.sqrt(v + eps)
def _scatter_add(tgt, idx, src, axis=0):
    return tgt.clone().index_add_(axis, idx, src)
def _segment_sum(src, seg, n):
    out = torch.zeros((n,) + src.shape[1:], dtype=src.dtype)
    return out.index_add_(0, seg, src)
def _pad(x, pw):
    flat = tuple(v for pair in reversed(pw) for v in pair)
    return torch.nn.functional.pad(x, flat)

kernel_name = 'standalone__gqa_block_cached__near_ridge'

def setup_inputs():

    _w = (lambda a: (lambda t: t.to(torch.bfloat16) if t.is_floating_point() else t)(torch.as_tensor(a)))
    B, Sq, D = 16, 96, 576
    Hq, Hkv, Sk, Dh = 32, 16, 512, 128
    assert Hq % Hkv == 0, (Hq, Hkv)
    return {
        "x": _w((lambda *s: torch.randn(*s, dtype=torch.bfloat16) * 0.8)(B, Sq, D)),
        "k_cache": _w((lambda *s: torch.randn(*s, dtype=torch.bfloat16) * 0.8)(B, Hkv, Sk, Dh)),
        "v_cache": _w((lambda *s: torch.randn(*s, dtype=torch.bfloat16) * 0.05)(B, Hkv, Sk, Dh)),
        "wq": _w((lambda *s: torch.randn(*s, dtype=torch.bfloat16) * 0.8)(D, Hq * Dh) * (D ** -0.5)),
        "wo": _w((lambda *s: torch.randn(*s, dtype=torch.bfloat16) * 0.05)(Hq * Dh, D) * ((Hq * Dh) ** -0.5)),
    }

def reference(x, k_cache, v_cache, wq, wo):
    B, Sq, D = x.shape
    Hq, Dh = 32, 128
    Hkv = k_cache.shape[1]
    rep = Hq // Hkv
    q = (lambda x, *d: x.permute(*d))((x @ wq).reshape(B, Sq, Hq, Dh), 0, 2, 1, 3)
    k = (lambda x, n, axis: torch.repeat_interleave(x, n, dim=axis))(k_cache, rep, axis=1)
    v = (lambda x, n, axis: torch.repeat_interleave(x, n, dim=axis))(v_cache, rep, axis=1)
    scores = torch.einsum("bhqd,bhkd->bhqk", q, k) * (Dh ** -0.5)
    p = _softmax(scores, axis=-1)
    o = torch.einsum("bhqk,bhkd->bhqd", p, v)
    return (lambda x, *d: x.permute(*d))(o, 0, 2, 1, 3).reshape(B, Sq, Hq * Dh) @ wo
```

```cpp
#include <hip/hip_runtime.h>
#include <hip/hip_bf16.h>
typedef unsigned short u16;
constexpr int B=16,SQ=96,D=576,HQ=32,HKV=16,SK=512,DH=128;
__device__ __forceinline__ float b2f(u16 v){return __uint_as_float(((unsigned)v)<<16);}
__device__ __forceinline__ u16 f2b(float f){unsigned u=__float_as_uint(f);u+=0x7fffu+((u>>16)&1u);return (u16)(u>>16);}
__global__ void gemm_naive(const u16* __restrict__ A,const u16* __restrict__ Bm,u16* __restrict__ C,int M,int N,int K){
  __shared__ float As[16][17]; __shared__ float Bs[16][17];
  int tx=threadIdx.x&15,ty=threadIdx.x>>4;
  int row=blockIdx.y*16+ty,col=blockIdx.x*16+tx;
  float acc=0;
  for(int k0=0;k0<K;k0+=16){
    As[ty][tx]=b2f(A[(size_t)row*K+k0+tx]);
    Bs[ty][tx]=b2f(Bm[(size_t)(k0+ty)*N+col]);
    __syncthreads();
    for(int k=0;k<16;k++)acc+=As[ty][k]*Bs[k][tx];
    __syncthreads();
  }
  C[(size_t)row*N+col]=f2b(acc);
}
__global__ void attn_naive(const u16* __restrict__ Q,const u16* __restrict__ K,const u16* __restrict__ V,u16* __restrict__ O){
  int id=blockIdx.x; int q=id%SQ; int h=(id/SQ)%HQ; int b=id/(SQ*HQ); int kvh=h/2;
  __shared__ float qs[DH]; __shared__ float p[SK]; __shared__ float red[256];
  int t=threadIdx.x;
  if(t<DH) qs[t]=b2f(Q[((size_t)(b*SQ+q))*HQ*DH+h*DH+t]);
  __syncthreads();
  const u16* Kb=K+((size_t)(b*HKV+kvh))*SK*DH; const u16* Vb=V+((size_t)(b*HKV+kvh))*SK*DH;
  float m=-1e30f;
  for(int k=t;k<SK;k+=256){float s=0;for(int d=0;d<DH;d++)s+=qs[d]*b2f(Kb[k*DH+d]); s*=0.08838834764f; p[k]=s; m=fmaxf(m,s);}
  red[t]=m; __syncthreads();
  for(int o=128;o>0;o>>=1){if(t<o)red[t]=fmaxf(red[t],red[t+o]);__syncthreads();}
  m=red[0]; __syncthreads();
  float l=0;
  for(int k=t;k<SK;k+=256){float e=__expf(p[k]-m);p[k]=e;l+=e;}
  red[t]=l; __syncthreads();
  for(int o=128;o>0;o>>=1){if(t<o)red[t]+=red[t+o];__syncthreads();}
  l=red[0];
  if(t<DH){float acc=0;for(int k=0;k<SK;k++)acc+=p[k]*b2f(Vb[k*DH+t]); O[((size_t)(b*SQ+q))*HQ*DH+h*DH+t]=f2b(acc/l);}
}
extern "C" void kernel_launch(void* const* d_in,const int* in_sizes,int n_in,void* d_out,int out_size,void* d_ws,size_t ws_size,hipStream_t stream){
  const u16* x=(const u16*)d_in[0];const u16* k=(const u16*)d_in[1];const u16* v=(const u16*)d_in[2];const u16* wq=(const u16*)d_in[3];const u16* wo=(const u16*)d_in[4];
  u16* Q=(u16*)d_ws; u16* O=Q+(size_t)B*SQ*HQ*DH;
  gemm_naive<<<dim3(HQ*DH/16,B*SQ/16),256,0,stream>>>(x,wq,Q,B*SQ,HQ*DH,D);
  attn_naive<<<B*HQ*SQ,256,0,stream>>>(Q,k,v,O);
  gemm_naive<<<dim3(D/16,B*SQ/16),256,0,stream>>>(O,wo,(u16*)d_out,B*SQ,D,HQ*DH);
}
```

```cpp
#include <hip/hip_runtime.h>
typedef unsigned u32;

#define VC8(a) "v" #a "0","v" #a "1","v" #a "2","v" #a "3","v" #a "4","v" #a "5","v" #a "6","v" #a "7","v" #a "8","v" #a "9"
#define SC8(a) "s" #a "0","s" #a "1","s" #a "2","s" #a "3","s" #a "4","s" #a "5","s" #a "6","s" #a "7","s" #a "8","s" #a "9"

__global__ __launch_bounds__(512, 2) void gqa_main(const void* x, const void* k, const void* v, const void* wq, const void* wo,
                                                   void* out, void* ws, void* pad) {
  __shared__ __attribute__((aligned(16))) char lds[163840];
  const u32 wid = __builtin_amdgcn_readfirstlane(threadIdx.x >> 6);
  asm volatile(
".Lg_start_%=:\n"
"s_load_dwordx16 s[12:27], %0, 0x0\n"
"s_and_b32 s62, %1, 7\n"
"s_lshr_b32 s63, %1, 3\n"
"s_and_b32 s64, s63, 1\n"
"s_lshr_b32 s58, s63, 1\n"
"s_lshl_b32 s66, s62, 1\n"
"s_add_u32 s57, s66, s64\n"
"s_mov_b32 s56, s58\n"
"s_lshl_b32 s66, s64, 2\n"
"s_add_u32 s59, s58, s66\n"
"s_cmp_ge_u32 s59, 9\n"
"s_cselect_b32 s66, 9, 0\n"
"s_sub_u32 s59, s59, s66\n"
"s_cmp_ge_u32 s59, 9\n"
"s_cselect_b32 s66, 9, 0\n"
"s_sub_u32 s59, s59, s66\n"
"s_lshl_b32 s66, s64, 2\n"
"s_add_u32 s59, s58, s66\n"
"s_cmp_ge_u32 s59, 6\n"
"s_cselect_b32 s66, 6, 0\n"
"s_sub_u32 s59, s59, s66\n"
"s_cmp_ge_u32 s59, 6\n"
"s_cselect_b32 s66, 6, 0\n"
"s_sub_u32 s59, s59, s66\n"
"s_cmp_ge_u32 s59, 6\n"
"s_cselect_b32 s66, 6, 0\n"
"s_sub_u32 s59, s59, s66\n"
"s_add_u32 s59, s59, 2\n"
"s_and_b32 s60, %2, 3\n"
"s_mov_b32 s61, 0x100000\n"
"v_mbcnt_lo_u32_b32 v255, -1, 0\n"
"v_mbcnt_hi_u32_b32 v255, -1, v255\n"
"s_lshl_b32 s62, s56, 4\n"
"s_add_u32 s62, s62, s57\n"
"s_cmp_lt_u32 %2, 4\n"
"s_cbranch_scc1 .Lg_compute_%=\n"
"v_lshrrev_b32 v8, 3, v255\n"
"v_and_b32 v9, 7, v255\n"
"v_lshrrev_b32 v10, 2, v8\n"
"v_and_b32 v11, 3, v8\n"
"v_mul_u32_u24 v10, 55296, v10\n"
"v_mul_u32_u24 v11, 4608, v11\n"
"v_add_u32 v10, v10, v11\n"
"v_lshl_add_u32 v1, v9, 4, v10\n"
"s_mul_i32 s63, s60, 1152\n"
"v_add_u32 v1, s63, v1\n"
"v_lshrrev_b32 v10, 5, v255\n"
"v_and_b32 v11, 31, v255\n"
"v_lshlrev_b32 v10, 15, v10\n"
"v_lshl_add_u32 v0, v11, 4, v10\n"
"s_lshl_b32 s63, s60, 16\n"
"v_add_u32 v0, s63, v0\n"
"s_mul_i32 s70, s60, 4352\n"
"s_and_b32 s63, s60, 1\n"
"s_lshl_b32 s63, s63, 3\n"
"s_add_u32 s70, s70, s63\n"
"s_add_u32 s70, s70, 33792\n"
"s_mul_i32 s71, s60, 1056\n"
"s_add_u32 s71, s71, 103424\n"
"s_mov_b32 s78, 0\n"
"s_mov_b32 s79, 0\n"
"s_mul_i32 s63, s56, 110592\n"
"s_lshl_b32 s64, s57, 9\n"
"s_sub_u32 s10, 4718592, s64\n"
"s_mov_b32 s11, 0x20000\n"
"s_mov_b32 s30, 110592\n"
"s_mov_b32 s31, 0x20000\n"
"s_waitcnt lgkmcnt(0)\n"
"s_add_u32 s28, s12, s63\n"
"s_addc_u32 s29, s13, 0\n"
"s_add_u32 s8, s18, s64\n"
"s_addc_u32 s9, s19, 0\n"
"s_lshl_b32 s74, s79, 7\n"
"s_add_u32 m0, s71, 0\n"
"s_add_u32 s67, s74, 0\n"
"buffer_load_dwordx4 v1, s[28:31], s67 offen lds\n"
"s_add_u32 m0, s71, 4224\n"
"s_add_u32 s67, s74, 18432\n"
"buffer_load_dwordx4 v1, s[28:31], s67 offen lds\n"
"s_add_u32 m0, s71, 8448\n"
"s_add_u32 s67, s74, 36864\n"
"buffer_load_dwordx4 v1, s[28:31], s67 offen lds\n"
"s_add_u32 s79, s79, 1\n"
"s_cmp_eq_u32 s79, 9\n"
"s_cselect_b32 s79, 3, s79\n"
"s_lshl_b32 s73, s78, 19\n"
"s_add_u32 m0, s70, 0\n"
"s_add_u32 s67, s73, 0\n"
"buffer_load_dwordx4 v0, s[8:11], s67 offen lds\n"
"s_add_u32 m0, s70, 1088\n"
"s_add_u32 s67, s73, 8192\n"
"buffer_load_dwordx4 v0, s[8:11], s67 offen lds\n"
"s_add_u32 m0, s70, 2176\n"
"s_add_u32 s67, s73, 16384\n"
"buffer_load_dwordx4 v0, s[8:11], s67 offen lds\n"
"s_add_u32 m0, s70, 3264\n"
"s_add_u32 s67, s73, 24576\n"
"buffer_load_dwordx4 v0, s[8:11], s67 offen lds\n"
"s_add_u32 m0, s70, 17408\n"
"s_add_u32 s67, s73, 262144\n"
"buffer_load_dwordx4 v0, s[8:11], s67 offen lds\n"
"s_add_u32 m0, s70, 18496\n"
"s_add_u32 s67, s73, 270336\n"
"buffer_load_dwordx4 v0, s[8:11], s67 offen lds\n"
"s_add_u32 m0, s70, 19584\n"
"s_add_u32 s67, s73, 278528\n"
"buffer_load_dwordx4 v0, s[8:11], s67 offen lds\n"
"s_add_u32 m0, s70, 20672\n"
"s_add_u32 s67, s73, 286720\n"
"buffer_load_dwordx4 v0, s[8:11], s67 offen lds\n"
"s_add_u32 s78, s78, 1\n"
"s_cmp_eq_u32 s78, 9\n"
"s_cselect_b32 s78, 3, s78\n"
"v_lshlrev_b32 v10, 7, v255\n"
"v_mov_b32 v11, 0x70000000\n"
"v_cmp_gt_u32 vcc, 32, v255\n"
"s_nop 1\n"
"v_cndmask_b32 v14, v11, v10, vcc\n"
"v_lshrrev_b32 v10, 4, v255\n"
"v_and_b32 v11, 15, v255\n"
"v_lshlrev_b32 v10, 11, v10\n"
"v_lshl_add_u32 v2, v11, 4, v10\n"
"s_lshl_b32 s63, s60, 9\n"
"v_add_u32 v2, s63, v2\n"
"v_mul_u32_u24 v10, 9216, v8\n"
"v_lshl_add_u32 v3, v9, 4, v10\n"
"s_mul_i32 s63, s60, 2304\n"
"v_add_u32 v3, s63, v3\n"
"s_lshl_b32 s63, s60, 8\n"
"s_add_u32 s63, s63, 160512\n"
"v_lshl_add_u32 v4, v255, 2, s63\n"
"v_and_b32 v10, 3, v255\n"
"v_lshlrev_b32 v10, 8, v10\n"
"v_add_u32 v5, 161536, v10\n"
"v_mov_b32 v6, 0\n"
"ds_write_b32 v4, v6\n"
"s_mul_i32 s72, s60, 2112\n"
"s_lshl_b32 s63, s62, 17\n"
"s_add_u32 s36, s14, s63\n"
"s_addc_u32 s37, s15, 0\n"
"s_mov_b32 s38, 131072\n"
"s_mov_b32 s39, 0x20000\n"
"s_add_u32 s40, s16, s63\n"
"s_addc_u32 s41, s17, 0\n"
"s_mov_b32 s42, 131072\n"
"s_mov_b32 s43, 0x20000\n"
"s_mul_i32 s63, s57, 294912\n"
"s_add_u32 s44, s20, s63\n"
"s_addc_u32 s45, s21, 0\n"
"s_mov_b32 s46, 294912\n"
"s_mov_b32 s47, 0x20000\n"
"s_waitcnt lgkmcnt(0)\n"
"s_lshl_b32 s74, s79, 7\n"
"s_add_u32 m0, s71, 12672\n"
"s_add_u32 s67, s74, 0\n"
"buffer_load_dwordx4 v1, s[28:31], s67 offen lds\n"
"s_add_u32 m0, s71, 16896\n"
"s_add_u32 s67, s74, 18432\n"
"buffer_load_dwordx4 v1, s[28:31], s67 offen lds\n"
"s_add_u32 m0, s71, 21120\n"
"s_add_u32 s67, s74, 36864\n"
"buffer_load_dwordx4 v1, s[28:31], s67 offen lds\n"
"s_add_u32 s79, s79, 1\n"
"s_cmp_eq_u32 s79, 9\n"
"s_cselect_b32 s79, 3, s79\n"
"s_lshl_b32 s73, s78, 19\n"
"s_add_u32 m0, s70, 34816\n"
"s_add_u32 s67, s73, 0\n"
"buffer_load_dwordx4 v0, s[8:11], s67 offen lds\n"
"s_add_u32 m0, s70, 35904\n"
"s_add_u32 s67, s73, 8192\n"
"buffer_load_dwordx4 v0, s[8:11], s67 offen lds\n"
"s_add_u32 m0, s70, 36992\n"
"s_add_u32 s67, s73, 16384\n"
"buffer_load_dwordx4 v0, s[8:11], s67 offen lds\n"
"s_add_u32 m0, s70, 38080\n"
"s_add_u32 s67, s73, 24576\n"
"buffer_load_dwordx4 v0, s[8:11], s67 offen lds\n"
"s_mov_b32 s82, 0\n"
"s_mov_b32 s83, 0\n"
"s_waitcnt vmcnt(11)\n"
"s_barrier\n"
"s_add_u32 m0, s70, 52224\n"
"s_add_u32 s67, s73, 262144\n"
"buffer_load_dwordx4 v0, s[8:11], s67 offen lds\n"
"s_add_u32 m0, s70, 53312\n"
"s_add_u32 s67, s73, 270336\n"
"buffer_load_dwordx4 v0, s[8:11], s67 offen lds\n"
"s_add_u32 m0, s70, 54400\n"
"s_add_u32 s67, s73, 278528\n"
"buffer_load_dwordx4 v0, s[8:11], s67 offen lds\n"
"s_add_u32 m0, s70, 55488\n"
"s_add_u32 s67, s73, 286720\n"
"buffer_load_dwordx4 v0, s[8:11], s67 offen lds\n"
"s_add_u32 s78, s78, 1\n"
"s_cmp_eq_u32 s78, 9\n"
"s_cselect_b32 s78, 3, s78\n"
"s_lshl_b32 s74, s79, 7\n"
"s_add_u32 m0, s71, 25344\n"
"s_add_u32 s67, s74, 0\n"
"buffer_load_dwordx4 v1, s[28:31], s67 offen lds\n"
"s_add_u32 m0, s71, 29568\n"
"s_add_u32 s67, s74, 18432\n"
"buffer_load_dwordx4 v1, s[28:31], s67 offen lds\n"
"s_add_u32 m0, s71, 33792\n"
"s_add_u32 s67, s74, 36864\n"
"buffer_load_dwordx4 v1, s[28:31], s67 offen lds\n"
"s_add_u32 s79, s59, 1\n"
"s_cmp_eq_u32 s79, 9\n"
"s_cselect_b32 s79, 3, s79\n"
"s_getpc_b64 s[48:49]\n"
".Lg_pc_%=:\n"
"s_sub_u32 s48, s48, .Lg_pc_%=-.Lg_start_%=\n"
"s_subb_u32 s49, s49, 0\n"
"s_mov_b32 s50, .Lg_end_%=-.Lg_start_%=\n"
"s_mov_b32 s51, 0x20000\n"
"s_and_b32 s63, s58, 1\n"
"s_lshl_b32 s63, s63, 2\n"
"s_add_u32 s63, s63, s60\n"
"s_lshl_b32 s63, s63, 12\n"
"buffer_load_dword v13, v14, s[48:51], s63 offen\n"
"s_lshl_b32 s65, s56, 7\n"
"s_add_u32 s65, s65, 35651584\n"
"s_add_u32 s80, s24, s65\n"
"s_addc_u32 s81, s25, 0\n"
"v_mov_b32 v18, 0\n"
"global_load_dwordx2 v[18:19], v18, s[80:81] sc1\n"
"v_lshlrev_b32 v8, 4, v255\n"
"v_add_u32 v8, s72, v8\n"
"v_add_u32 v9, 67584, v8\n"
"s_mov_b32 s66, 0\n"
".Lg_wloop_%=:\n"
"s_waitcnt vmcnt(14)\n"
"s_barrier\n"
"s_lshl_b32 s73, s78, 19\n"
"s_add_u32 s77, s70, s82\n"
"s_add_u32 m0, s77, 0\n"
"s_add_u32 s67, s73, 0\n"
"buffer_load_dwordx4 v0, s[8:11], s67 offen lds\n"
"s_add_u32 m0, s77, 1088\n"
"s_add_u32 s67, s73, 8192\n"
"buffer_load_dwordx4 v0, s[8:11], s67 offen lds\n"
"s_add_u32 m0, s77, 2176\n"
"s_add_u32 s67, s73, 16384\n"
"buffer_load_dwordx4 v0, s[8:11], s67 offen lds\n"
"s_add_u32 m0, s77, 3264\n"
"s_add_u32 s67, s73, 24576\n"
"buffer_load_dwordx4 v0, s[8:11], s67 offen lds\n"
"s_add_u32 s82, s82, 17408\n"
"s_cmp_eq_u32 s82, 69632\n"
"s_cselect_b32 s82, 0, s82\n"
"s_waitcnt vmcnt(11)\n"
"s_barrier\n"
"s_add_u32 s77, s70, s82\n"
"s_add_u32 m0, s77, 0\n"
"s_add_u32 s67, s73, 262144\n"
"buffer_load_dwordx4 v0, s[8:11], s67 offen lds\n"
"s_add_u32 m0, s77, 1088\n"
"s_add_u32 s67, s73, 270336\n"
"buffer_load_dwordx4 v0, s[8:11], s67 offen lds\n"
"s_add_u32 m0, s77, 2176\n"
"s_add_u32 s67, s73, 278528\n"
"buffer_load_dwordx4 v0, s[8:11], s67 offen lds\n"
"s_add_u32 m0, s77, 3264\n"
"s_add_u32 s67, s73, 286720\n"
"buffer_load_dwordx4 v0, s[8:11], s67 offen lds\n"
"s_add_u32 s82, s82, 17408\n"
"s_cmp_eq_u32 s82, 69632\n"
"s_cselect_b32 s82, 0, s82\n"
"s_cmp_eq_u32 s66, 0\n"
"s_cselect_b32 s78, s59, s78\n"
"s_add_u32 s78, s78, 1\n"
"s_cmp_eq_u32 s78, 9\n"
"s_cselect_b32 s78, 3, s78\n"
"s_cmp_eq_u32 s66, 6\n"
"s_cbranch_scc1 .Lg_wdone_%=\n"
"s_lshl_b32 s74, s79, 7\n"
"s_add_u32 s77, s71, s83\n"
"s_add_u32 m0, s77, 0\n"
"s_add_u32 s67, s74, 0\n"
"buffer_load_dwordx4 v1, s[28:31], s67 offen lds\n"
"s_add_u32 m0, s77, 4224\n"
"s_add_u32 s67, s74, 18432\n"
"buffer_load_dwordx4 v1, s[28:31], s67 offen lds\n"
"s_add_u32 m0, s77, 8448\n"
"s_add_u32 s67, s74, 36864\n"
"buffer_load_dwordx4 v1, s[28:31], s67 offen lds\n"
"s_add_u32 s83, s83, 12672\n"
"s_cmp_eq_u32 s83, 38016\n"
"s_cselect_b32 s83, 0, s83\n"
"s_add_u32 s79, s79, 1\n"
"s_cmp_eq_u32 s79, 9\n"
"s_cselect_b32 s79, 3, s79\n"
"s_add_u32 s66, s66, 1\n"
"s_branch .Lg_wloop_%=\n"
".Lg_wdone_%=:\n"
"s_cmp_ge_u32 s56, 9\n"
"s_cselect_b32 s64, 9, 0\n"
"s_sub_u32 s64, s56, s64\n"
"s_add_u32 m0, s72, 152064\n"
"s_mov_b32 s67, 0\n"
"buffer_load_dwordx4 v2, s[36:39], s67 offen nt lds\n"
"s_add_u32 m0, s72, 153120\n"
"s_mov_b32 s67, 256\n"
"buffer_load_dwordx4 v2, s[36:39], s67 offen nt lds\n"
"s_lshl_b32 s12, s64, 7\n"
"s_add_u32 s64, s64, 1\n"
"s_cmp_eq_u32 s64, 9\n"
"s_cselect_b32 s64, 0, s64\n"
"s_lshl_b32 s13, s64, 7\n"
"s_add_u32 s64, s64, 1\n"
"s_cmp_eq_u32 s64, 9\n"
"s_cselect_b32 s64, 0, s64\n"
"s_lshl_b32 s14, s64, 7\n"
"s_add_u32 s64, s64, 1\n"
"s_cmp_eq_u32 s64, 9\n"
"s_cselect_b32 s64, 0, s64\n"
"s_lshl_b32 s15, s64, 7\n"
"s_add_u32 s64, s64, 1\n"
"s_cmp_eq_u32 s64, 9\n"
"s_cselect_b32 s64, 0, s64\n"
"s_lshl_b32 s16, s64, 7\n"
"s_add_u32 s64, s64, 1\n"
"s_cmp_eq_u32 s64, 9\n"
"s_cselect_b32 s64, 0, s64\n"
"s_lshl_b32 s17, s64, 7\n"
"s_add_u32 s64, s64, 1\n"
"s_cmp_eq_u32 s64, 9\n"
"s_cselect_b32 s64, 0, s64\n"
"s_lshl_b32 s18, s64, 7\n"
"s_add_u32 s64, s64, 1\n"
"s_cmp_eq_u32 s64, 9\n"
"s_cselect_b32 s64, 0, s64\n"
"s_lshl_b32 s19, s64, 7\n"
"s_add_u32 s64, s64, 1\n"
"s_cmp_eq_u32 s64, 9\n"
"s_cselect_b32 s64, 0, s64\n"
"s_lshl_b32 s20, s64, 7\n"
"s_add_u32 s64, s64, 1\n"
"s_cmp_eq_u32 s64, 9\n"
"s_cselect_b32 s64, 0, s64\n"
"s_waitcnt vmcnt(13)\n"
"s_barrier\n"
"s_add_u32 m0, s72, 8448\n"
"s_mov_b32 s67, 8192\n"
"buffer_load_dwordx4 v2, s[36:39], s67 offen nt lds\n"
"s_add_u32 m0, s72, 9504\n"
"s_mov_b32 s67, 8448\n"
"buffer_load_dwordx4 v2, s[36:39], s67 offen nt lds\n"
"s_add_u32 m0, s72, 0\n"
"s_mov_b32 s67, 0\n"
"buffer_load_dwordx4 v2, s[40:43], s67 offen nt lds\n"
"s_add_u32 m0, s72, 1056\n"
"s_mov_b32 s67, 256\n"
"buffer_load_dwordx4 v2, s[40:43], s67 offen nt lds\n"
"s_waitcnt vmcnt(10)\n"
"s_barrier\n"
"s_add_u32 m0, s72, 25344\n"
"s_mov_b32 s67, 16384\n"
"buffer_load_dwordx4 v2, s[36:39], s67 offen nt lds\n"
"s_add_u32 m0, s72, 26400\n"
"s_mov_b32 s67, 16640\n"
"buffer_load_dwordx4 v2, s[36:39], s67 offen nt lds\n"
"s_add_u32 m0, s72, 16896\n"
"s_mov_b32 s67, 8192\n"
"buffer_load_dwordx4 v2, s[40:43], s67 offen nt lds\n"
"s_add_u32 m0, s72, 17952\n"
"s_mov_b32 s67, 8448\n"
"buffer_load_dwordx4 v2, s[40:43], s67 offen nt lds\n"
"s_waitcnt vmcnt(10)\n"
"s_barrier\n"
"s_barrier\n"
"s_waitcnt vmcnt(8)\n"
"v_mov_b32 v6, 1\n"
"ds_max_u32 v4, v6\n"
"s_add_u32 m0, s72, 42240\n"
"s_mov_b32 s67, 24576\n"
"buffer_load_dwordx4 v2, s[36:39], s67 offen nt lds\n"
"s_add_u32 m0, s72, 43296\n"
"s_mov_b32 s67, 24832\n"
"buffer_load_dwordx4 v2, s[36:39], s67 offen nt lds\n"
"s_add_u32 m0, s72, 33792\n"
"s_mov_b32 s67, 16384\n"
"buffer_load_dwordx4 v2, s[40:43], s67 offen nt lds\n"
"s_add_u32 m0, s72, 34848\n"
"s_mov_b32 s67, 16640\n"
"buffer_load_dwordx4 v2, s[40:43], s67 offen nt lds\n"
"s_waitcnt vmcnt(8)\n"
"v_mov_b32 v6, 2\n"
"ds_max_u32 v4, v6\n"
"s_add_u32 m0, s72, 59136\n"
"s_mov_b32 s67, 32768\n"
"buffer_load_dwordx4 v2, s[36:39], s67 offen nt lds\n"
"s_add_u32 m0, s72, 60192\n"
"s_mov_b32 s67, 33024\n"
"buffer_load_dwordx4 v2, s[36:39], s67 offen nt lds\n"
"s_add_u32 m0, s72, 50688\n"
"s_mov_b32 s67, 24576\n"
"buffer_load_dwordx4 v2, s[40:43], s67 offen nt lds\n"
"s_add_u32 m0, s72, 51744\n"
"s_mov_b32 s67, 24832\n"
"buffer_load_dwordx4 v2, s[40:43], s67 offen nt lds\n"
"s_waitcnt vmcnt(8)\n"
"v_mov_b32 v6, 3\n"
"ds_max_u32 v4, v6\n"
"s_add_u32 m0, s72, 76032\n"
"s_mov_b32 s67, 40960\n"
"buffer_load_dwordx4 v2, s[36:39], s67 offen nt lds\n"
"s_add_u32 m0, s72, 77088\n"
"s_mov_b32 s67, 41216\n"
"buffer_load_dwordx4 v2, s[36:39], s67 offen nt lds\n"
"s_add_u32 m0, s72, 67584\n"
"s_mov_b32 s67, 32768\n"
"buffer_load_dwordx4 v2, s[40:43], s67 offen nt lds\n"
"s_add_u32 m0, s72, 68640\n"
"s_mov_b32 s67, 33024\n"
"buffer_load_dwordx4 v2, s[40:43], s67 offen nt lds\n"
"s_waitcnt vmcnt(8)\n"
"v_mov_b32 v6, 4\n"
"ds_max_u32 v4, v6\n"
"s_add_u32 m0, s72, 92928\n"
"s_mov_b32 s67, 49152\n"
"buffer_load_dwordx4 v2, s[36:39], s67 offen nt lds\n"
"s_add_u32 m0, s72, 93984\n"
"s_mov_b32 s67, 49408\n"
"buffer_load_dwordx4 v2, s[36:39], s67 offen nt lds\n"
"s_add_u32 m0, s72, 84480\n"
"s_mov_b32 s67, 40960\n"
"buffer_load_dwordx4 v2, s[40:43], s67 offen nt lds\n"
"s_add_u32 m0, s72, 85536\n"
"s_mov_b32 s67, 41216\n"
"buffer_load_dwordx4 v2, s[40:43], s67 offen nt lds\n"
"s_waitcnt vmcnt(8)\n"
"v_mov_b32 v6, 5\n"
"ds_max_u32 v4, v6\n"
"s_add_u32 m0, s72, 109824\n"
"s_mov_b32 s67, 57344\n"
"buffer_load_dwordx4 v2, s[36:39], s67 offen nt lds\n"
"s_add_u32 m0, s72, 110880\n"
"s_mov_b32 s67, 57600\n"
"buffer_load_dwordx4 v2, s[36:39], s67 offen nt lds\n"
"s_add_u32 m0, s72, 101376\n"
"s_mov_b32 s67, 49152\n"
"buffer_load_dwordx4 v2, s[40:43], s67 offen nt lds\n"
"s_add_u32 m0, s72, 102432\n"
"s_mov_b32 s67, 49408\n"
"buffer_load_dwordx4 v2, s[40:43], s67 offen nt lds\n"
"s_waitcnt vmcnt(8)\n"
"v_mov_b32 v6, 6\n"
"ds_max_u32 v4, v6\n"
"s_add_u32 m0, s72, 126720\n"
"s_mov_b32 s67, 65536\n"
"buffer_load_dwordx4 v2, s[36:39], s67 offen nt lds\n"
"s_add_u32 m0, s72, 127776\n"
"s_mov_b32 s67, 65792\n"
"buffer_load_dwordx4 v2, s[36:39], s67 offen nt lds\n"
"s_add_u32 m0, s72, 118272\n"
"s_mov_b32 s67, 57344\n"
"buffer_load_dwordx4 v2, s[40:43], s67 offen nt lds\n"
"s_add_u32 m0, s72, 119328\n"
"s_mov_b32 s67, 57600\n"
"buffer_load_dwordx4 v2, s[40:43], s67 offen nt lds\n"
"s_waitcnt vmcnt(8)\n"
"v_mov_b32 v6, 7\n"
"ds_max_u32 v4, v6\n"
"s_add_u32 m0, s72, 143616\n"
"s_mov_b32 s67, 73728\n"
"buffer_load_dwordx4 v2, s[36:39], s67 offen nt lds\n"
"s_add_u32 m0, s72, 144672\n"
"s_mov_b32 s67, 73984\n"
"buffer_load_dwordx4 v2, s[36:39], s67 offen nt lds\n"
"s_add_u32 m0, s72, 135168\n"
"s_mov_b32 s67, 65536\n"
"buffer_load_dwordx4 v2, s[40:43], s67 offen nt lds\n"
"s_add_u32 m0, s72, 136224\n"
"s_mov_b32 s67, 65792\n"
"buffer_load_dwordx4 v2, s[40:43], s67 offen nt lds\n"
"ds_read_b32 v7, v5\n"
"s_waitcnt vmcnt(8)\n"
"v_mov_b32 v6, 8\n"
"ds_max_u32 v4, v6\n"
"s_waitcnt lgkmcnt(1)\n"
"v_cmp_gt_i32 vcc, 1, v7\n"
"s_cbranch_vccnz .Lg_park21_%=\n"
".Lg_go22_%=:\n"
"s_add_u32 m0, s72, 8448\n"
"s_mov_b32 s67, 81920\n"
"buffer_load_dwordx4 v2, s[36:39], s67 offen nt lds\n"
"s_add_u32 m0, s72, 9504\n"
"s_mov_b32 s67, 82176\n"
"buffer_load_dwordx4 v2, s[36:39], s67 offen nt lds\n"
"s_add_u32 m0, s72, 0\n"
"s_mov_b32 s67, 73728\n"
"buffer_load_dwordx4 v2, s[40:43], s67 offen nt lds\n"
"s_add_u32 m0, s72, 1056\n"
"s_mov_b32 s67, 73984\n"
"buffer_load_dwordx4 v2, s[40:43], s67 offen nt lds\n"
"ds_read_b32 v7, v5\n"
"s_waitcnt vmcnt(8)\n"
"v_mov_b32 v6, 9\n"
"ds_max_u32 v4, v6\n"
"s_waitcnt lgkmcnt(1)\n"
"v_cmp_gt_i32 vcc, 2, v7\n"
"s_cbranch_vccnz .Lg_park24_%=\n"
".Lg_go25_%=:\n"
"s_add_u32 m0, s72, 25344\n"
"s_mov_b32 s67, 90112\n"
"buffer_load_dwordx4 v2, s[36:39], s67 offen nt lds\n"
"s_add_u32 m0, s72, 26400\n"
"s_mov_b32 s67, 90368\n"
"buffer_load_dwordx4 v2, s[36:39], s67 offen nt lds\n"
"s_add_u32 m0, s72, 16896\n"
"s_mov_b32 s67, 81920\n"
"buffer_load_dwordx4 v2, s[40:43], s67 offen nt lds\n"
"s_add_u32 m0, s72, 17952\n"
"s_mov_b32 s67, 82176\n"
"buffer_load_dwordx4 v2, s[40:43], s67 offen nt lds\n"
"ds_read_b32 v7, v5\n"
"s_waitcnt vmcnt(8)\n"
"v_mov_b32 v6, 10\n"
"ds_max_u32 v4, v6\n"
"s_waitcnt lgkmcnt(1)\n"
"v_cmp_gt_i32 vcc, 3, v7\n"
"s_cbranch_vccnz .Lg_park27_%=\n"
".Lg_go28_%=:\n"
"s_add_u32 m0, s72, 42240\n"
"s_mov_b32 s67, 98304\n"
"buffer_load_dwordx4 v2, s[36:39], s67 offen nt lds\n"
"s_add_u32 m0, s72, 43296\n"
"s_mov_b32 s67, 98560\n"
"buffer_load_dwordx4 v2, s[36:39], s67 offen nt lds\n"
"s_add_u32 m0, s72, 33792\n"
"s_mov_b32 s67, 90112\n"
"buffer_load_dwordx4 v2, s[40:43], s67 offen nt lds\n"
"s_add_u32 m0, s72, 34848\n"
"s_mov_b32 s67, 90368\n"
"buffer_load_dwordx4 v2, s[40:43], s67 offen nt lds\n"
"ds_read_b32 v7, v5\n"
"s_waitcnt vmcnt(8)\n"
"v_mov_b32 v6, 11\n"
"ds_max_u32 v4, v6\n"
"s_waitcnt lgkmcnt(1)\n"
"v_cmp_gt_i32 vcc, 4, v7\n"
"s_cbranch_vccnz .Lg_park30_%=\n"
".Lg_go31_%=:\n"
"s_add_u32 m0, s72, 59136\n"
"s_mov_b32 s67, 106496\n"
"buffer_load_dwordx4 v2, s[36:39], s67 offen nt lds\n"
"s_add_u32 m0, s72, 60192\n"
"s_mov_b32 s67, 106752\n"
"buffer_load_dwordx4 v2, s[36:39], s67 offen nt lds\n"
"s_add_u32 m0, s72, 50688\n"
"s_mov_b32 s67, 98304\n"
"buffer_load_dwordx4 v2, s[40:43], s67 offen nt lds\n"
"s_add_u32 m0, s72, 51744\n"
"s_mov_b32 s67, 98560\n"
"buffer_load_dwordx4 v2, s[40:43], s67 offen nt lds\n"
"ds_read_b32 v7, v5\n"
"s_waitcnt vmcnt(8)\n"
"v_mov_b32 v6, 12\n"
"ds_max_u32 v4, v6\n"
"s_waitcnt lgkmcnt(1)\n"
"v_cmp_gt_i32 vcc, 5, v7\n"
"s_cbranch_vccnz .Lg_park33_%=\n"
".Lg_go34_%=:\n"
"s_add_u32 m0, s72, 76032\n"
"s_mov_b32 s67, 114688\n"
"buffer_load_dwordx4 v2, s[36:39], s67 offen nt lds\n"
"s_add_u32 m0, s72, 77088\n"
"s_mov_b32 s67, 114944\n"
"buffer_load_dwordx4 v2, s[36:39], s67 offen nt lds\n"
"s_add_u32 m0, s72, 67584\n"
"s_mov_b32 s67, 106496\n"
"buffer_load_dwordx4 v2, s[40:43], s67 offen nt lds\n"
"s_add_u32 m0, s72, 68640\n"
"s_mov_b32 s67, 106752\n"
"buffer_load_dwordx4 v2, s[40:43], s67 offen nt lds\n"
"ds_read_b32 v7, v5\n"
"s_waitcnt vmcnt(8)\n"
"v_mov_b32 v6, 13\n"
"ds_max_u32 v4, v6\n"
"s_waitcnt lgkmcnt(1)\n"
"v_cmp_gt_i32 vcc, 6, v7\n"
"s_cbranch_vccnz .Lg_park36_%=\n"
".Lg_go37_%=:\n"
"s_add_u32 m0, s72, 92928\n"
"s_mov_b32 s67, 122880\n"
"buffer_load_dwordx4 v2, s[36:39], s67 offen nt lds\n"
"s_add_u32 m0, s72, 93984\n"
"s_mov_b32 s67, 123136\n"
"buffer_load_dwordx4 v2, s[36:39], s67 offen nt lds\n"
"s_add_u32 m0, s72, 84480\n"
"s_mov_b32 s67, 114688\n"
"buffer_load_dwordx4 v2, s[40:43], s67 offen nt lds\n"
"s_add_u32 m0, s72, 85536\n"
"s_mov_b32 s67, 114944\n"
"buffer_load_dwordx4 v2, s[40:43], s67 offen nt lds\n"
"ds_read_b32 v7, v5\n"
"s_waitcnt vmcnt(8)\n"
"v_mov_b32 v6, 14\n"
"ds_max_u32 v4, v6\n"
"s_waitcnt lgkmcnt(1)\n"
"v_cmp_gt_i32 vcc, 7, v7\n"
"s_cbranch_vccnz .Lg_park39_%=\n"
".Lg_go40_%=:\n"
"s_add_u32 m0, s72, 101376\n"
"s_mov_b32 s67, 122880\n"
"buffer_load_dwordx4 v2, s[40:43], s67 offen nt lds\n"
"s_add_u32 m0, s72, 102432\n"
"s_mov_b32 s67, 123136\n"
"buffer_load_dwordx4 v2, s[40:43], s67 offen nt lds\n"
"ds_read_b32 v7, v5\n"
"s_waitcnt vmcnt(8)\n"
"s_waitcnt lgkmcnt(0)\n"
"v_cmp_gt_i32 vcc, 11, v7\n"
"s_cbranch_vccnz .Lg_park42_%=\n"
".Lg_go43_%=:\n"
"s_add_u32 m0, s72, 0\n"
"s_add_u32 s67, s12, 0\n"
"buffer_load_dwordx4 v3, s[44:47], s67 offen lds\n"
"s_add_u32 m0, s72, 1056\n"
"s_add_u32 s67, s12, 1152\n"
"buffer_load_dwordx4 v3, s[44:47], s67 offen lds\n"
"s_add_u32 m0, s72, 8448\n"
"s_add_u32 s67, s12, 73728\n"
"buffer_load_dwordx4 v3, s[44:47], s67 offen lds\n"
"s_add_u32 m0, s72, 9504\n"
"s_add_u32 s67, s12, 74880\n"
"buffer_load_dwordx4 v3, s[44:47], s67 offen lds\n"
"s_waitcnt vmcnt(8)\n"
"v_mov_b32 v6, 15\n"
"ds_max_u32 v4, v6\n"
"s_add_u32 m0, s72, 16896\n"
"s_add_u32 s67, s12, 147456\n"
"buffer_load_dwordx4 v3, s[44:47], s67 offen lds\n"
"s_add_u32 m0, s72, 17952\n"
"s_add_u32 s67, s12, 148608\n"
"buffer_load_dwordx4 v3, s[44:47], s67 offen lds\n"
"s_add_u32 m0, s72, 25344\n"
"s_add_u32 s67, s12, 221184\n"
"buffer_load_dwordx4 v3, s[44:47], s67 offen lds\n"
"s_add_u32 m0, s72, 26400\n"
"s_add_u32 s67, s12, 222336\n"
"buffer_load_dwordx4 v3, s[44:47], s67 offen lds\n"
"s_waitcnt vmcnt(8)\n"
"v_mov_b32 v6, 17\n"
"ds_max_u32 v4, v6\n"
"s_add_u32 s67, s15, 0\n"
"buffer_load_dwordx4 v[20:23], v3, s[44:47], s67 offen\n"
"s_add_u32 s67, s15, 1152\n"
"buffer_load_dwordx4 v[24:27], v3, s[44:47], s67 offen\n"
"s_add_u32 s67, s15, 73728\n"
"buffer_load_dwordx4 v[28:31], v3, s[44:47], s67 offen\n"
"s_add_u32 s67, s15, 74880\n"
"buffer_load_dwordx4 v[32:35], v3, s[44:47], s67 offen\n"
"s_waitcnt vmcnt(8)\n"
"s_add_u32 s67, s15, 147456\n"
"buffer_load_dwordx4 v[36:39], v3, s[44:47], s67 offen\n"
"s_add_u32 s67, s15, 148608\n"
"buffer_load_dwordx4 v[40:43], v3, s[44:47], s67 offen\n"
"s_add_u32 s67, s15, 221184\n"
"buffer_load_dwordx4 v[44:47], v3, s[44:47], s67 offen\n"
"s_add_u32 s67, s15, 222336\n"
"buffer_load_dwordx4 v[48:51], v3, s[44:47], s67 offen\n"
"s_waitcnt vmcnt(8)\n"
"v_mov_b32 v6, 18\n"
"ds_max_u32 v4, v6\n"
"s_add_u32 s67, s16, 0\n"
"buffer_load_dwordx4 v[52:55], v3, s[44:47], s67 offen\n"
"s_add_u32 s67, s16, 1152\n"
"buffer_load_dwordx4 v[56:59], v3, s[44:47], s67 offen\n"
"s_add_u32 s67, s16, 73728\n"
"buffer_load_dwordx4 v[60:63], v3, s[44:47], s67 offen\n"
"s_add_u32 s67, s16, 74880\n"
"buffer_load_dwordx4 v[64:67], v3, s[44:47], s67 offen\n"
"s_waitcnt vmcnt(8)\n"
"s_add_u32 s67, s16, 147456\n"
"buffer_load_dwordx4 v[68:71], v3, s[44:47], s67 offen\n"
"s_add_u32 s67, s16, 148608\n"
"buffer_load_dwordx4 v[72:75], v3, s[44:47], s67 offen\n"
"s_add_u32 s67, s16, 221184\n"
"buffer_load_dwordx4 v[76:79], v3, s[44:47], s67 offen\n"
"s_add_u32 s67, s16, 222336\n"
"buffer_load_dwordx4 v[80:83], v3, s[44:47], s67 offen\n"
"s_waitcnt vmcnt(8)\n"
"s_add_u32 s67, s17, 0\n"
"buffer_load_dwordx4 v[84:87], v3, s[44:47], s67 offen\n"
"s_add_u32 s67, s17, 1152\n"
"buffer_load_dwordx4 v[88:91], v3, s[44:47], s67 offen\n"
"s_add_u32 s67, s17, 73728\n"
"buffer_load_dwordx4 v[92:95], v3, s[44:47], s67 offen\n"
"s_add_u32 s67, s17, 74880\n"
"buffer_load_dwordx4 v[96:99], v3, s[44:47], s67 offen\n"
"s_waitcnt vmcnt(8)\n"
"s_add_u32 s67, s17, 147456\n"
"buffer_load_dwordx4 v[100:103], v3, s[44:47], s67 offen\n"
"s_add_u32 s67, s17, 148608\n"
"buffer_load_dwordx4 v[104:107], v3, s[44:47], s67 offen\n"
"s_add_u32 s67, s17, 221184\n"
"buffer_load_dwordx4 v[108:111], v3, s[44:47], s67 offen\n"
"s_add_u32 s67, s17, 222336\n"
"buffer_load_dwordx4 v[112:115], v3, s[44:47], s67 offen\n"
"ds_read_b32 v7, v5\n"
"s_waitcnt vmcnt(8)\n"
"s_waitcnt lgkmcnt(0)\n"
"v_cmp_gt_i32 vcc, 13, v7\n"
"s_cbranch_vccnz .Lg_park45_%=\n"
".Lg_go46_%=:\n"
"s_add_u32 m0, s72, 33792\n"
"s_add_u32 s67, s13, 0\n"
"buffer_load_dwordx4 v3, s[44:47], s67 offen lds\n"
"s_add_u32 m0, s72, 34848\n"
"s_add_u32 s67, s13, 1152\n"
"buffer_load_dwordx4 v3, s[44:47], s67 offen lds\n"
"s_add_u32 m0, s72, 42240\n"
"s_add_u32 s67, s13, 73728\n"
"buffer_load_dwordx4 v3, s[44:47], s67 offen lds\n"
"s_add_u32 m0, s72, 43296\n"
"s_add_u32 s67, s13, 74880\n"
"buffer_load_dwordx4 v3, s[44:47], s67 offen lds\n"
"s_waitcnt vmcnt(8)\n"
"s_add_u32 m0, s72, 50688\n"
"s_add_u32 s67, s13, 147456\n"
"buffer_load_dwordx4 v3, s[44:47], s67 offen lds\n"
"s_add_u32 m0, s72, 51744\n"
"s_add_u32 s67, s13, 148608\n"
"buffer_load_dwordx4 v3, s[44:47], s67 offen lds\n"
"s_add_u32 m0, s72, 59136\n"
"s_add_u32 s67, s13, 221184\n"
"buffer_load_dwordx4 v3, s[44:47], s67 offen lds\n"
"s_add_u32 m0, s72, 60192\n"
"s_add_u32 s67, s13, 222336\n"
"buffer_load_dwordx4 v3, s[44:47], s67 offen lds\n"
"s_waitcnt vmcnt(8)\n"
"s_add_u32 s67, s18, 0\n"
"buffer_load_dwordx4 v[116:119], v3, s[44:47], s67 offen\n"
"s_add_u32 s67, s18, 1152\n"
"buffer_load_dwordx4 v[120:123], v3, s[44:47], s67 offen\n"
"s_add_u32 s67, s18, 73728\n"
"buffer_load_dwordx4 v[124:127], v3, s[44:47], s67 offen\n"
"s_add_u32 s67, s18, 74880\n"
"buffer_load_dwordx4 v[128:131], v3, s[44:47], s67 offen\n"
"s_waitcnt vmcnt(8)\n"
"s_add_u32 s67, s18, 147456\n"
"buffer_load_dwordx4 v[132:135], v3, s[44:47], s67 offen\n"
"s_add_u32 s67, s18, 148608\n"
"buffer_load_dwordx4 v[136:139], v3, s[44:47], s67 offen\n"
"s_add_u32 s67, s18, 221184\n"
"buffer_load_dwordx4 v[140:143], v3, s[44:47], s67 offen\n"
"s_add_u32 s67, s18, 222336\n"
"buffer_load_dwordx4 v[144:147], v3, s[44:47], s67 offen\n"
"s_waitcnt vmcnt(8)\n"
"v_mov_b32 v6, 19\n"
"ds_max_u32 v4, v6\n"
"s_add_u32 s67, s19, 0\n"
"buffer_load_dwordx4 v[148:151], v3, s[44:47], s67 offen\n"
"s_add_u32 s67, s19, 1152\n"
"buffer_load_dwordx4 v[152:155], v3, s[44:47], s67 offen\n"
"s_add_u32 s67, s19, 73728\n"
"buffer_load_dwordx4 v[156:159], v3, s[44:47], s67 offen\n"
"s_add_u32 s67, s19, 74880\n"
"buffer_load_dwordx4 v[160:163], v3, s[44:47], s67 offen\n"
"s_waitcnt vmcnt(8)\n"
"s_add_u32 s67, s19, 147456\n"
"buffer_load_dwordx4 v[164:167], v3, s[44:47], s67 offen\n"
"s_add_u32 s67, s19, 148608\n"
"buffer_load_dwordx4 v[168:171], v3, s[44:47], s67 offen\n"
"s_add_u32 s67, s19, 221184\n"
"buffer_load_dwordx4 v[172:175], v3, s[44:47], s67 offen\n"
"s_add_u32 s67, s19, 222336\n"
"buffer_load_dwordx4 v[176:179], v3, s[44:47], s67 offen\n"
"s_waitcnt vmcnt(8)\n"
"s_add_u32 s67, s20, 0\n"
"buffer_load_dwordx4 v[180:183], v3, s[44:47], s67 offen\n"
"s_add_u32 s67, s20, 1152\n"
"buffer_load_dwordx4 v[184:187], v3, s[44:47], s67 offen\n"
"s_add_u32 s67, s20, 73728\n"
"buffer_load_dwordx4 v[188:191], v3, s[44:47], s67 offen\n"
"s_add_u32 s67, s20, 74880\n"
"buffer_load_dwordx4 v[192:195], v3, s[44:47], s67 offen\n"
"s_waitcnt vmcnt(8)\n"
"s_add_u32 s67, s20, 147456\n"
"buffer_load_dwordx4 v[196:199], v3, s[44:47], s67 offen\n"
"s_add_u32 s67, s20, 148608\n"
"buffer_load_dwordx4 v[200:203], v3, s[44:47], s67 offen\n"
"s_add_u32 s67, s20, 221184\n"
"buffer_load_dwordx4 v[204:207], v3, s[44:47], s67 offen\n"
"s_add_u32 s67, s20, 222336\n"
"buffer_load_dwordx4 v[208:211], v3, s[44:47], s67 offen\n"
"ds_read_b32 v7, v5\n"
"s_waitcnt vmcnt(8)\n"
"s_waitcnt lgkmcnt(0)\n"
"v_cmp_gt_i32 vcc, 15, v7\n"
"s_cbranch_vccnz .Lg_park48_%=\n"
".Lg_go49_%=:\n"
"s_add_u32 m0, s72, 67584\n"
"s_add_u32 s67, s14, 0\n"
"buffer_load_dwordx4 v3, s[44:47], s67 offen lds\n"
"s_add_u32 m0, s72, 68640\n"
"s_add_u32 s67, s14, 1152\n"
"buffer_load_dwordx4 v3, s[44:47], s67 offen lds\n"
"s_add_u32 m0, s72, 76032\n"
"s_add_u32 s67, s14, 73728\n"
"buffer_load_dwordx4 v3, s[44:47], s67 offen lds\n"
"s_add_u32 m0, s72, 77088\n"
"s_add_u32 s67, s14, 74880\n"
"buffer_load_dwordx4 v3, s[44:47], s67 offen lds\n"
"s_waitcnt vmcnt(8)\n"
"s_add_u32 m0, s72, 84480\n"
"s_add_u32 s67, s14, 147456\n"
"buffer_load_dwordx4 v3, s[44:47], s67 offen lds\n"
"s_add_u32 m0, s72, 85536\n"
"s_add_u32 s67, s14, 148608\n"
"buffer_load_dwordx4 v3, s[44:47], s67 offen lds\n"
"s_add_u32 m0, s72, 92928\n"
"s_add_u32 s67, s14, 221184\n"
"buffer_load_dwordx4 v3, s[44:47], s67 offen lds\n"
"s_add_u32 m0, s72, 93984\n"
"s_add_u32 s67, s14, 222336\n"
"buffer_load_dwordx4 v3, s[44:47], s67 offen lds\n"
"v_readfirstlane_b32 s80, v18\n"
"v_readfirstlane_b32 s81, v19\n"
"s_mul_i32 s65, s80, 0x9e3779b1\n"
"s_add_u32 s65, s65, s81\n"
"s_add_u32 s65, s65, 0x7f4a7c15\n"
"s_lshr_b32 s66, s65, 15\n"
"s_xor_b32 s65, s65, s66\n"
"s_mul_i32 s65, s65, 0x85ebca77\n"
"s_lshr_b32 s66, s65, 13\n"
"s_xor_b32 s65, s65, s66\n"
"s_xor_b32 s66, s81, s65\n"
"s_mul_i32 s66, s66, 0xc2b2ae3d\n"
"s_add_u32 s66, s66, s80\n"
"s_mov_b32 s81, s65\n"
"s_lshr_b32 s65, s66, 16\n"
"s_xor_b32 s66, s66, s65\n"
"s_mul_i32 s66, s66, 0x165667b1\n"
"s_lshr_b32 s65, s66, 15\n"
"s_xor_b32 s66, s66, s65\n"
"s_and_b32 s80, s66, 0xfffffff0\n"
"s_mul_i32 s63, s56, 1769472\n"
"s_add_u32 s28, s24, s63\n"
"s_addc_u32 s29, s25, 0\n"
"s_mov_b32 s30, 1769472\n"
"s_mov_b32 s31, 0x20000\n"
"s_mul_i32 s63, s56, 110592\n"
"s_add_u32 s8, s22, s63\n"
"s_addc_u32 s9, s23, 0\n"
"s_mov_b32 s10, 110592\n"
"s_mov_b32 s11, 0x20000\n"
"s_lshl_b32 s63, s62, 12\n"
"s_add_u32 s63, s63, 33554432\n"
"s_add_u32 s36, s24, s63\n"
"s_addc_u32 s37, s25, 0\n"
"s_mov_b32 s38, 4096\n"
"s_mov_b32 s39, 0x20000\n"
"s_lshl_b32 s63, s56, 16\n"
"s_add_u32 s63, s63, 33554432\n"
"s_add_u32 s40, s24, s63\n"
"s_addc_u32 s41, s25, 0\n"
"s_mov_b32 s42, 65536\n"
"s_mov_b32 s43, 0x20000\n"
"v_mov_b32 v3, 0x70000000\n"
"s_cmp_ge_u32 s56, 9\n"
"s_cselect_b32 s64, 9, 0\n"
"s_sub_u32 s64, s56, s64\n"
"s_lshr_b32 s63, s57, 1\n"
"s_mul_i32 s63, s63, 3\n"
"s_and_b32 s66, s57, 1\n"
"v_lshrrev_b32 v238, 5, v255\n"
"s_sub_u32 s65, 3, s60\n"
"v_lshl_add_u32 v238, v238, 2, s65\n"
"v_mul_u32_u24 v240, 43, v238\n"
"v_lshrrev_b32 v240, 7, v240\n"
"v_mul_u32_u24 v241, 3, v240\n"
"v_sub_u32 v237, v238, v241\n"
"v_add_u32 v241, s66, v237\n"
"v_lshrrev_b32 v239, 1, v241\n"
"v_lshl_add_u32 v236, v240, 1, v239\n"
"s_lshl_b32 s66, s66, 1\n"
"v_add_u32 v237, s66, v237\n"
"v_subrev_u32 v241, 3, v237\n"
"v_min_u32 v237, v237, v241\n"
"v_add_u32 v240, s63, v237\n"
"v_and_b32 v238, 15, v255\n"
"v_lshlrev_b32 v235, 3, v238\n"
"v_and_b32 v241, 3, v238\n"
"v_lshlrev_b32 v234, 4, v241\n"
"v_lshrrev_b32 v241, 2, v238\n"
"v_mul_u32_u24 v241, 128, v241\n"
"v_add_u32 v234, v234, v241\n"
"v_bfe_u32 v238, v255, 4, 1\n"
"v_lshl_add_u32 v234, v238, 6, v234\n"
"v_lshl_add_u32 v234, v240, 9, v234\n"
"v_mul_u32_u24 v241, 43, v240\n"
"v_lshrrev_b32 v241, 7, v241\n"
"v_lshl_add_u32 v238, v241, 1, v238\n"
"v_mul_u32_u24 v241, 3, v241\n"
"v_sub_u32 v240, v240, v241\n"
"v_lshl_add_u32 v238, v240, 5, v238\n"
"v_mul_u32_u24 v238, 1152, v238\n"
"v_add_u32 v235, v235, v238\n"
"s_add_u32 s65, s64, 0\n"
"v_add_u32 v239, s65, v236\n"
"v_subrev_u32 v240, 9, v239\n"
"v_min_u32 v239, v239, v240\n"
"v_cmp_gt_u32 vcc, 4, v236\n"
"v_mul_u32_u24 v240, 196608, v239\n"
"v_add_u32 v212, v234, v240\n"
"v_lshl_add_u32 v213, v239, 7, v235\n"
"v_cndmask_b32 v212, v3, v212, vcc\n"
"v_cndmask_b32 v213, v3, v213, vcc\n"
"s_add_u32 s65, s64, 4\n"
"v_add_u32 v239, s65, v236\n"
"v_subrev_u32 v240, 9, v239\n"
"v_min_u32 v239, v239, v240\n"
"v_cmp_gt_u32 vcc, 5, v236\n"
"v_mul_u32_u24 v240, 196608, v239\n"
"v_add_u32 v214, v234, v240\n"
"v_lshl_add_u32 v215, v239, 7, v235\n"
"v_cndmask_b32 v214, v3, v214, vcc\n"
"v_cndmask_b32 v215, v3, v215, vcc\n"
"s_and_b32 s64, s60, 1\n"
"v_and_b32 v15, 15, v255\n"
"v_lshrrev_b32 v3, 4, v255\n"
"v_lshlrev_b32 v3, 8, v3\n"
"v_lshl_add_u32 v15, v15, 12, v3\n"
"s_lshl_b32 s65, s57, 3\n"
"v_add_u32 v15, s65, v15\n"
"v_add_u32 v3, 16, v255\n"
"v_and_b32 v3, 63, v3\n"
"v_cmp_gt_u32 vcc, 16, v3\n"
"v_mov_b32 v3, 0x70000000\n"
"s_nop 0\n"
"v_cndmask_b32 v15, v3, v15, vcc\n"
"v_mov_b32 v19, s81\n"
"s_waitcnt vmcnt(0)\n"
"v_mov_b32 v6, 20\n"
"ds_max_u32 v4, v6\n"
"ds_read_b32 v7, v5\n"
"s_waitcnt lgkmcnt(0)\n"
"v_cmp_gt_i32 vcc, 18, v7\n"
"s_cbranch_vccnz .Lg_park51_%=\n"
".Lg_go52_%=:\n"
"ds_write_b128 v8, v[20:23] offset:0\n"
"ds_write_b128 v8, v[24:27] offset:1056\n"
"ds_write_b128 v8, v[28:31] offset:8448\n"
"ds_write_b128 v8, v[32:35] offset:9504\n"
"ds_write_b128 v8, v[36:39] offset:16896\n"
"ds_write_b128 v8, v[40:43] offset:17952\n"
"ds_write_b128 v8, v[44:47] offset:25344\n"
"ds_write_b128 v8, v[48:51] offset:26400\n"
"s_waitcnt lgkmcnt(0)\n"
"v_mov_b32 v6, 21\n"
"ds_max_u32 v4, v6\n"
"ds_read_b32 v7, v5\n"
"s_waitcnt lgkmcnt(0)\n"
"v_cmp_gt_i32 vcc, 19, v7\n"
"s_cbranch_vccnz .Lg_park54_%=\n"
".Lg_go55_%=:\n"
"ds_write_b128 v8, v[52:55] offset:33792\n"
"ds_write_b128 v8, v[56:59] offset:34848\n"
"ds_write_b128 v8, v[60:63] offset:42240\n"
"ds_write_b128 v8, v[64:67] offset:43296\n"
"ds_write_b128 v8, v[68:71] offset:50688\n"
"ds_write_b128 v8, v[72:75] offset:51744\n"
"ds_write_b128 v8, v[76:79] offset:59136\n"
"ds_write_b128 v8, v[80:83] offset:60192\n"
"s_waitcnt lgkmcnt(0)\n"
"v_mov_b32 v6, 22\n"
"ds_max_u32 v4, v6\n"
"ds_read_b32 v7, v5\n"
"s_waitcnt lgkmcnt(0)\n"
"v_cmp_gt_i32 vcc, 20, v7\n"
"s_cbranch_vccnz .Lg_park57_%=\n"
".Lg_go58_%=:\n"
"ds_write_b128 v9, v[84:87] offset:0\n"
"ds_write_b128 v9, v[88:91] offset:1056\n"
"ds_write_b128 v9, v[92:95] offset:8448\n"
"ds_write_b128 v9, v[96:99] offset:9504\n"
"ds_write_b128 v9, v[100:103] offset:16896\n"
"ds_write_b128 v9, v[104:107] offset:17952\n"
"ds_write_b128 v9, v[108:111] offset:25344\n"
"ds_write_b128 v9, v[112:115] offset:26400\n"
"s_waitcnt lgkmcnt(0)\n"
"v_mov_b32 v6, 23\n"
"ds_max_u32 v4, v6\n"
"ds_read_b32 v7, v5\n"
"s_waitcnt lgkmcnt(0)\n"
"v_cmp_gt_i32 vcc, 21, v7\n"
"s_cbranch_vccnz .Lg_park60_%=\n"
".Lg_go61_%=:\n"
"ds_write_b128 v8, v[116:119] offset:0\n"
"ds_write_b128 v8, v[120:123] offset:1056\n"
"ds_write_b128 v8, v[124:127] offset:8448\n"
"ds_write_b128 v8, v[128:131] offset:9504\n"
"ds_write_b128 v8, v[132:135] offset:16896\n"
"ds_write_b128 v8, v[136:139] offset:17952\n"
"ds_write_b128 v8, v[140:143] offset:25344\n"
"ds_write_b128 v8, v[144:147] offset:26400\n"
"s_waitcnt lgkmcnt(0)\n"
"v_mov_b32 v6, 24\n"
"ds_max_u32 v4, v6\n"
"ds_read_b32 v7, v5\n"
"s_waitcnt lgkmcnt(0)\n"
"v_cmp_gt_i32 vcc, 22, v7\n"
"s_cbranch_vccnz .Lg_park63_%=\n"
".Lg_go64_%=:\n"
"ds_write_b128 v8, v[148:151] offset:33792\n"
"ds_write_b128 v8, v[152:155] offset:34848\n"
"ds_write_b128 v8, v[156:159] offset:42240\n"
"ds_write_b128 v8, v[160:163] offset:43296\n"
"ds_write_b128 v8, v[164:167] offset:50688\n"
"ds_write_b128 v8, v[168:171] offset:51744\n"
"ds_write_b128 v8, v[172:175] offset:59136\n"
"ds_write_b128 v8, v[176:179] offset:60192\n"
"s_waitcnt lgkmcnt(0)\n"
"v_mov_b32 v6, 25\n"
"ds_max_u32 v4, v6\n"
"ds_read_b32 v7, v5\n"
"s_waitcnt lgkmcnt(0)\n"
"v_cmp_gt_i32 vcc, 23, v7\n"
"s_cbranch_vccnz .Lg_park66_%=\n"
".Lg_go67_%=:\n"
"ds_write_b128 v9, v[180:183] offset:0\n"
"ds_write_b128 v9, v[184:187] offset:1056\n"
"ds_write_b128 v9, v[188:191] offset:8448\n"
"ds_write_b128 v9, v[192:195] offset:9504\n"
"ds_write_b128 v9, v[196:199] offset:16896\n"
"ds_write_b128 v9, v[200:203] offset:17952\n"
"ds_write_b128 v9, v[204:207] offset:25344\n"
"ds_write_b128 v9, v[208:211] offset:26400\n"
"s_waitcnt lgkmcnt(0)\n"
"v_mov_b32 v6, 26\n"
"ds_max_u32 v4, v6\n"
"s_mov_b32 s83, 0xffff0000\n"
"v_mov_b32 v7, v5\n"
"s_cmp_eq_u32 s60, 1\n"
"s_cbranch_scc1 .Lg_poller_%=\n"
"s_cmp_lg_u32 s60, 0\n"
"s_cbranch_scc1 .Lg_wfin_%=\n"
".Lg_rown69_%=:\n"
"s_sub_u32 s61, s61, 1\n"
"s_cbranch_scc1 .Lg_exit_%=\n"
"ds_read_b32 v3, v7\n"
"s_waitcnt lgkmcnt(0)\n"
"v_cmp_gt_i32 vcc, 24, v3\n"
"s_cbranch_vccz .Lg_rgo70_%=\n"
"s_sleep 1\n"
"s_branch .Lg_rown69_%=\n"
".Lg_rgo70_%=:\n"
"s_add_u32 s63, s80, 1\n"
"v_mov_b32 v18, s63\n"
"buffer_store_dwordx2 v[18:19], v15, s[40:43], 0 offen offset:0 sc1\n"
".Lg_rown71_%=:\n"
"s_sub_u32 s61, s61, 1\n"
"s_cbranch_scc1 .Lg_exit_%=\n"
"ds_read_b32 v3, v7\n"
"s_waitcnt lgkmcnt(0)\n"
"v_cmp_gt_i32 vcc, 28, v3\n"
"s_cbranch_vccz .Lg_rgo72_%=\n"
"s_branch .Lg_rown71_%=\n"
".Lg_rgo72_%=:\n"
"s_add_u32 s63, s80, 2\n"
"v_mov_b32 v18, s63\n"
"buffer_store_dwordx2 v[18:19], v15, s[40:43], 0 offen offset:1024 sc1\n"
"s_branch .Lg_jobs_%=\n"
".Lg_poller_%=:\n"
".Lg_rown73_%=:\n"
"s_sub_u32 s61, s61, 1\n"
"s_cbranch_scc1 .Lg_exit_%=\n"
"ds_read_b32 v3, v7\n"
"s_waitcnt lgkmcnt(0)\n"
"v_cmp_gt_i32 vcc, 26, v3\n"
"s_cbranch_vccz .Lg_rgo74_%=\n"
"s_sleep 1\n"
"s_branch .Lg_rown73_%=\n"
".Lg_rgo74_%=:\n"
"s_add_u32 s78, s80, 1\n"
"s_mov_b32 s79, s81\n"
"s_mov_b32 s65, 0\n"
".Lg_rown78_%=:\n"
"s_sub_u32 s61, s61, 1\n"
"s_cbranch_scc1 .Lg_exit_%=\n"
"ds_read_b32 v3, v7\n"
"s_waitcnt lgkmcnt(0)\n"
"v_cmp_gt_i32 vcc, 27, v3\n"
"s_cbranch_vccz .Lg_rgo79_%=\n"
"s_branch .Lg_rown78_%=\n"
".Lg_rgo79_%=:\n"
"v_mbcnt_lo_u32_b32 v3, -1, 0\n"
"v_mbcnt_hi_u32_b32 v3, -1, v3\n"
"v_and_b32 v3, 15, v3\n"
"v_lshlrev_b32 v3, 3, v3\n"
".Lg_pvec77_%=:\n"
"buffer_load_dwordx2 v[234:235], v3, s[36:39], 0 offen offset:768 sc1\n"
"s_sub_u32 s61, s61, 16\n"
"s_cbranch_scc1 .Lg_exit_%=\n"
"s_waitcnt vmcnt(0)\n"
"v_cmp_ne_u64 vcc, s[78:79], v[234:235]\n"
"s_cbranch_vccnz .Lg_pvec77_%=\n"
".Lg_pok76_%=:\n"
"v_mov_b32 v6, 64\n"
"ds_max_u32 v4, v6\n"
"v_mov_b32 v234, 0\n"
"v_mov_b32 v235, 0\n"
"buffer_store_dwordx2 v[234:235], v3, s[36:39], 0 offen offset:768 sc1\n"
"s_branch .Lg_jobs_%=\n"
".Lg_wfin_%=:\n"
".Lg_rown80_%=:\n"
"s_sub_u32 s61, s61, 1\n"
"s_cbranch_scc1 .Lg_exit_%=\n"
"ds_read_b32 v3, v7\n"
"s_waitcnt lgkmcnt(0)\n"
"v_cmp_gt_i32 vcc, 27, v3\n"
"s_cbranch_vccz .Lg_rgo81_%=\n"
"s_sleep 1\n"
"s_branch .Lg_rown80_%=\n"
".Lg_rgo81_%=:\n"
".Lg_jobs_%=:\n"
"v_mov_b32 v7, 160768\n"
"s_mov_b32 s64, 64\n"
".Lg_prime_%=:\n"
".Lg_rown82_%=:\n"
"s_sub_u32 s61, s61, 1\n"
"s_cbranch_scc1 .Lg_exit_%=\n"
"ds_read_b32 v3, v7\n"
"s_waitcnt lgkmcnt(0)\n"
"v_cmp_gt_i32 vcc, s64, v3\n"
"s_cbranch_vccz .Lg_rgo83_%=\n"
"s_branch .Lg_rown82_%=\n"
".Lg_rgo83_%=:\n"
"s_mov_b32 s63, 0\n"
"buffer_load_dwordx4 v[20:23], v212, s[28:31], s63 offen sc1\n"
"s_add_u32 s63, s63, 12288\n"
"buffer_load_dwordx4 v[24:27], v212, s[28:31], s63 offen sc1\n"
"s_add_u32 s63, s63, 12288\n"
"buffer_load_dwordx4 v[28:31], v212, s[28:31], s63 offen sc1\n"
"s_add_u32 s63, s63, 12288\n"
"buffer_load_dwordx4 v[32:35], v212, s[28:31], s63 offen sc1\n"
"s_add_u32 s63, s63, 12288\n"
"buffer_load_dwordx4 v[36:39], v212, s[28:31], s63 offen sc1\n"
"s_add_u32 s63, s63, 12288\n"
"buffer_load_dwordx4 v[40:43], v212, s[28:31], s63 offen sc1\n"
"s_add_u32 s63, s63, 12288\n"
"buffer_load_dwordx4 v[44:47], v212, s[28:31], s63 offen sc1\n"
"s_add_u32 s63, s63, 12288\n"
"buffer_load_dwordx4 v[48:51], v212, s[28:31], s63 offen sc1\n"
"s_add_u32 s63, s63, 12288\n"
"buffer_load_dwordx4 v[52:55], v212, s[28:31], s63 offen sc1\n"
"s_add_u32 s63, s63, 12288\n"
"buffer_load_dwordx4 v[56:59], v212, s[28:31], s63 offen sc1\n"
"s_add_u32 s63, s63, 12288\n"
"buffer_load_dwordx4 v[60:63], v212, s[28:31], s63 offen sc1\n"
"s_add_u32 s63, s63, 12288\n"
"buffer_load_dwordx4 v[64:67], v212, s[28:31], s63 offen sc1\n"
"s_add_u32 s63, s63, 12288\n"
"buffer_load_dwordx4 v[68:71], v212, s[28:31], s63 offen sc1\n"
"s_add_u32 s63, s63, 12288\n"
"buffer_load_dwordx4 v[72:75], v212, s[28:31], s63 offen sc1\n"
"s_add_u32 s63, s63, 12288\n"
"buffer_load_dwordx4 v[76:79], v212, s[28:31], s63 offen sc1\n"
"s_add_u32 s63, s63, 12288\n"
"buffer_load_dwordx4 v[80:83], v212, s[28:31], s63 offen sc1\n"
"ds_read_b32 v3, v7\n"
".Lg_next_%=:\n"
"v_mov_b32 v85, v213\n"
"v_mov_b32 v212, v214\n"
"v_mov_b32 v213, v215\n"
"s_add_u32 s64, s64, 1\n"
"v_mov_b32 v84, 0x70000000\n"
"s_mov_b32 s66, 0\n"
"s_cmp_gt_u32 s64, 65\n"
"s_cbranch_scc1 .Lg_sum_%=\n"
"s_waitcnt lgkmcnt(0)\n"
"v_cmp_gt_i32 vcc, s64, v3\n"
"s_cbranch_vccnz .Lg_sum_%=\n"
"v_mov_b32 v84, v212\n"
"s_mov_b32 s66, 1\n"
".Lg_sum_%=:\n"
"s_mov_b32 s63, 0\n"
"s_waitcnt vmcnt(15)\n"
"v_lshlrev_b32 v242, 16, v20\n"
"v_and_b32 v243, s83, v20\n"
"v_lshlrev_b32 v244, 16, v21\n"
"v_and_b32 v245, s83, v21\n"
"v_lshlrev_b32 v246, 16, v22\n"
"v_and_b32 v247, s83, v22\n"
"v_lshlrev_b32 v248, 16, v23\n"
"v_and_b32 v249, s83, v23\n"
"buffer_load_dwordx4 v[20:23], v84, s[28:31], s63 offen sc1\n"
"s_add_u32 s63, s63, 12288\n"
"s_waitcnt vmcnt(15)\n"
"v_lshlrev_b32 v234, 16, v24\n"
"v_and_b32 v235, s83, v24\n"
"v_lshlrev_b32 v236, 16, v25\n"
"v_and_b32 v237, s83, v25\n"
"v_lshlrev_b32 v238, 16, v26\n"
"v_and_b32 v239, s83, v26\n"
"v_lshlrev_b32 v240, 16, v27\n"
"v_and_b32 v241, s83, v27\n"
"buffer_load_dwordx4 v[24:27], v84, s[28:31], s63 offen sc1\n"
"s_add_u32 s63, s63, 12288\n"
"v_pk_add_f32 v[242:243], v[242:243], v[234:235]\n"
"v_pk_add_f32 v[244:245], v[244:245], v[236:237]\n"
"v_pk_add_f32 v[246:247], v[246:247], v[238:239]\n"
"v_pk_add_f32 v[248:249], v[248:249], v[240:241]\n"
"s_waitcnt vmcnt(15)\n"
"v_lshlrev_b32 v234, 16, v28\n"
"v_and_b32 v235, s83, v28\n"
"v_lshlrev_b32 v236, 16, v29\n"
"v_and_b32 v237, s83, v29\n"
"v_lshlrev_b32 v238, 16, v30\n"
"v_and_b32 v239, s83, v30\n"
"v_lshlrev_b32 v240, 16, v31\n"
"v_and_b32 v241, s83, v31\n"
"buffer_load_dwordx4 v[28:31], v84, s[28:31], s63 offen sc1\n"
"s_add_u32 s63, s63, 12288\n"
"v_pk_add_f32 v[242:243], v[242:243], v[234:235]\n"
"v_pk_add_f32 v[244:245], v[244:245], v[236:237]\n"
"v_pk_add_f32 v[246:247], v[246:247], v[238:239]\n"
"v_pk_add_f32 v[248:249], v[248:249], v[240:241]\n"
"s_waitcnt vmcnt(15)\n"
"v_lshlrev_b32 v234, 16, v32\n"
"v_and_b32 v235, s83, v32\n"
"v_lshlrev_b32 v236, 16, v33\n"
"v_and_b32 v237, s83, v33\n"
"v_lshlrev_b32 v238, 16, v34\n"
"v_and_b32 v239, s83, v34\n"
"v_lshlrev_b32 v240, 16, v35\n"
"v_and_b32 v241, s83, v35\n"
"buffer_load_dwordx4 v[32:35], v84, s[28:31], s63 offen sc1\n"
"s_add_u32 s63, s63, 12288\n"
"v_pk_add_f32 v[242:243], v[242:243], v[234:235]\n"
"v_pk_add_f32 v[244:245], v[244:245], v[236:237]\n"
"v_pk_add_f32 v[246:247], v[246:247], v[238:239]\n"
"v_pk_add_f32 v[248:249], v[248:249], v[240:241]\n"
"s_waitcnt vmcnt(15)\n"
"v_lshlrev_b32 v234, 16, v36\n"
"v_and_b32 v235, s83, v36\n"
"v_lshlrev_b32 v236, 16, v37\n"
"v_and_b32 v237, s83, v37\n"
"v_lshlrev_b32 v238, 16, v38\n"
"v_and_b32 v239, s83, v38\n"
"v_lshlrev_b32 v240, 16, v39\n"
"v_and_b32 v241, s83, v39\n"
"buffer_load_dwordx4 v[36:39], v84, s[28:31], s63 offen sc1\n"
"s_add_u32 s63, s63, 12288\n"
"v_pk_add_f32 v[242:243], v[242:243], v[234:235]\n"
"v_pk_add_f32 v[244:245], v[244:245], v[236:237]\n"
"v_pk_add_f32 v[246:247], v[246:247], v[238:239]\n"
"v_pk_add_f32 v[248:249], v[248:249], v[240:241]\n"
"s_waitcnt vmcnt(15)\n"
"v_lshlrev_b32 v234, 16, v40\n"
"v_and_b32 v235, s83, v40\n"
"v_lshlrev_b32 v236, 16, v41\n"
"v_and_b32 v237, s83, v41\n"
"v_lshlrev_b32 v238, 16, v42\n"
"v_and_b32 v239, s83, v42\n"
"v_lshlrev_b32 v240, 16, v43\n"
"v_and_b32 v241, s83, v43\n"
"buffer_load_dwordx4 v[40:43], v84, s[28:31], s63 offen sc1\n"
"s_add_u32 s63, s63, 12288\n"
"v_pk_add_f32 v[242:243], v[242:243], v[234:235]\n"
"v_pk_add_f32 v[244:245], v[244:245], v[236:237]\n"
"v_pk_add_f32 v[246:247], v[246:247], v[238:239]\n"
"v_pk_add_f32 v[248:249], v[248:249], v[240:241]\n"
"s_waitcnt vmcnt(15)\n"
"v_lshlrev_b32 v234, 16, v44\n"
"v_and_b32 v235, s83, v44\n"
"v_lshlrev_b32 v236, 16, v45\n"
"v_and_b32 v237, s83, v45\n"
"v_lshlrev_b32 v238, 16, v46\n"
"v_and_b32 v239, s83, v46\n"
"v_lshlrev_b32 v240, 16, v47\n"
"v_and_b32 v241, s83, v47\n"
"buffer_load_dwordx4 v[44:47], v84, s[28:31], s63 offen sc1\n"
"s_add_u32 s63, s63, 12288\n"
"v_pk_add_f32 v[242:243], v[242:243], v[234:235]\n"
"v_pk_add_f32 v[244:245], v[244:245], v[236:237]\n"
"v_pk_add_f32 v[246:247], v[246:247], v[238:239]\n"
"v_pk_add_f32 v[248:249], v[248:249], v[240:241]\n"
"s_waitcnt vmcnt(15)\n"
"v_lshlrev_b32 v234, 16, v48\n"
"v_and_b32 v235, s83, v48\n"
"v_lshlrev_b32 v236, 16, v49\n"
"v_and_b32 v237, s83, v49\n"
"v_lshlrev_b32 v238, 16, v50\n"
"v_and_b32 v239, s83, v50\n"
"v_lshlrev_b32 v240, 16, v51\n"
"v_and_b32 v241, s83, v51\n"
"buffer_load_dwordx4 v[48:51], v84, s[28:31], s63 offen sc1\n"
"s_add_u32 s63, s63, 12288\n"
"v_pk_add_f32 v[242:243], v[242:243], v[234:235]\n"
"v_pk_add_f32 v[244:245], v[244:245], v[236:237]\n"
"v_pk_add_f32 v[246:247], v[246:247], v[238:239]\n"
"v_pk_add_f32 v[248:249], v[248:249], v[240:241]\n"
"s_waitcnt vmcnt(15)\n"
"v_lshlrev_b32 v234, 16, v52\n"
"v_and_b32 v235, s83, v52\n"
"v_lshlrev_b32 v236, 16, v53\n"
"v_and_b32 v237, s83, v53\n"
"v_lshlrev_b32 v238, 16, v54\n"
"v_and_b32 v239, s83, v54\n"
"v_lshlrev_b32 v240, 16, v55\n"
"v_and_b32 v241, s83, v55\n"
"buffer_load_dwordx4 v[52:55], v84, s[28:31], s63 offen sc1\n"
"s_add_u32 s63, s63, 12288\n"
"v_pk_add_f32 v[242:243], v[242:243], v[234:235]\n"
"v_pk_add_f32 v[244:245], v[244:245], v[236:237]\n"
"v_pk_add_f32 v[246:247], v[246:247], v[238:239]\n"
"v_pk_add_f32 v[248:249], v[248:249], v[240:241]\n"
"s_waitcnt vmcnt(15)\n"
"v_lshlrev_b32 v234, 16, v56\n"
"v_and_b32 v235, s83, v56\n"
"v_lshlrev_b32 v236, 16, v57\n"
"v_and_b32 v237, s83, v57\n"
"v_lshlrev_b32 v238, 16, v58\n"
"v_and_b32 v239, s83, v58\n"
"v_lshlrev_b32 v240, 16, v59\n"
"v_and_b32 v241, s83, v59\n"
"buffer_load_dwordx4 v[56:59], v84, s[28:31], s63 offen sc1\n"
"s_add_u32 s63, s63, 12288\n"
"v_pk_add_f32 v[242:243], v[242:243], v[234:235]\n"
"v_pk_add_f32 v[244:245], v[244:245], v[236:237]\n"
"v_pk_add_f32 v[246:247], v[246:247], v[238:239]\n"
"v_pk_add_f32 v[248:249], v[248:249], v[240:241]\n"
"s_waitcnt vmcnt(15)\n"
"v_lshlrev_b32 v234, 16, v60\n"
"v_and_b32 v235, s83, v60\n"
"v_lshlrev_b32 v236, 16, v61\n"
"v_and_b32 v237, s83, v61\n"
"v_lshlrev_b32 v238, 16, v62\n"
"v_and_b32 v239, s83, v62\n"
"v_lshlrev_b32 v240, 16, v63\n"
"v_and_b32 v241, s83, v63\n"
"buffer_load_dwordx4 v[60:63], v84, s[28:31], s63 offen sc1\n"
"s_add_u32 s63, s63, 12288\n"
"v_pk_add_f32 v[242:243], v[242:243], v[234:235]\n"
"v_pk_add_f32 v[244:245], v[244:245], v[236:237]\n"
"v_pk_add_f32 v[246:247], v[246:247], v[238:239]\n"
"v_pk_add_f32 v[248:249], v[248:249], v[240:241]\n"
"s_waitcnt vmcnt(15)\n"
"v_lshlrev_b32 v234, 16, v64\n"
"v_and_b32 v235, s83, v64\n"
"v_lshlrev_b32 v236, 16, v65\n"
"v_and_b32 v237, s83, v65\n"
"v_lshlrev_b32 v238, 16, v66\n"
"v_and_b32 v239, s83, v66\n"
"v_lshlrev_b32 v240, 16, v67\n"
"v_and_b32 v241, s83, v67\n"
"buffer_load_dwordx4 v[64:67], v84, s[28:31], s63 offen sc1\n"
"s_add_u32 s63, s63, 12288\n"
"v_pk_add_f32 v[242:243], v[242:243], v[234:235]\n"
"v_pk_add_f32 v[244:245], v[244:245], v[236:237]\n"
"v_pk_add_f32 v[246:247], v[246:247], v[238:239]\n"
"v_pk_add_f32 v[248:249], v[248:249], v[240:241]\n"
"s_waitcnt vmcnt(15)\n"
"v_lshlrev_b32 v234, 16, v68\n"
"v_and_b32 v235, s83, v68\n"
"v_lshlrev_b32 v236, 16, v69\n"
"v_and_b32 v237, s83, v69\n"
"v_lshlrev_b32 v238, 16, v70\n"
"v_and_b32 v239, s83, v70\n"
"v_lshlrev_b32 v240, 16, v71\n"
"v_and_b32 v241, s83, v71\n"
"buffer_load_dwordx4 v[68:71], v84, s[28:31], s63 offen sc1\n"
"s_add_u32 s63, s63, 12288\n"
"v_pk_add_f32 v[242:243], v[242:243], v[234:235]\n"
"v_pk_add_f32 v[244:245], v[244:245], v[236:237]\n"
"v_pk_add_f32 v[246:247], v[246:247], v[238:239]\n"
"v_pk_add_f32 v[248:249], v[248:249], v[240:241]\n"
"s_waitcnt vmcnt(15)\n"
"v_lshlrev_b32 v234, 16, v72\n"
"v_and_b32 v235, s83, v72\n"
"v_lshlrev_b32 v236, 16, v73\n"
"v_and_b32 v237, s83, v73\n"
"v_lshlrev_b32 v238, 16, v74\n"
"v_and_b32 v239, s83, v74\n"
"v_lshlrev_b32 v240, 16, v75\n"
"v_and_b32 v241, s83, v75\n"
"buffer_load_dwordx4 v[72:75], v84, s[28:31], s63 offen sc1\n"
"s_add_u32 s63, s63, 12288\n"
"v_pk_add_f32 v[242:243], v[242:243], v[234:235]\n"
"v_pk_add_f32 v[244:245], v[244:245], v[236:237]\n"
"v_pk_add_f32 v[246:247], v[246:247], v[238:239]\n"
"v_pk_add_f32 v[248:249], v[248:249], v[240:241]\n"
"ds_read_b32 v3, v7\n"
"s_waitcnt vmcnt(15)\n"
"v_lshlrev_b32 v234, 16, v76\n"
"v_and_b32 v235, s83, v76\n"
"v_lshlrev_b32 v236, 16, v77\n"
"v_and_b32 v237, s83, v77\n"
"v_lshlrev_b32 v238, 16, v78\n"
"v_and_b32 v239, s83, v78\n"
"v_lshlrev_b32 v240, 16, v79\n"
"v_and_b32 v241, s83, v79\n"
"buffer_load_dwordx4 v[76:79], v84, s[28:31], s63 offen sc1\n"
"s_add_u32 s63, s63, 12288\n"
"v_pk_add_f32 v[242:243], v[242:243], v[234:235]\n"
"v_pk_add_f32 v[244:245], v[244:245], v[236:237]\n"
"v_pk_add_f32 v[246:247], v[246:247], v[238:239]\n"
"v_pk_add_f32 v[248:249], v[248:249], v[240:241]\n"
"s_waitcnt vmcnt(15)\n"
"v_lshlrev_b32 v234, 16, v80\n"
"v_and_b32 v235, s83, v80\n"
"v_lshlrev_b32 v236, 16, v81\n"
"v_and_b32 v237, s83, v81\n"
"v_lshlrev_b32 v238, 16, v82\n"
"v_and_b32 v239, s83, v82\n"
"v_lshlrev_b32 v240, 16, v83\n"
"v_and_b32 v241, s83, v83\n"
"buffer_load_dwordx4 v[80:83], v84, s[28:31], s63 offen sc1\n"
"v_pk_add_f32 v[242:243], v[242:243], v[234:235]\n"
"v_pk_add_f32 v[244:245], v[244:245], v[236:237]\n"
"v_pk_add_f32 v[246:247], v[246:247], v[238:239]\n"
"v_pk_add_f32 v[248:249], v[248:249], v[240:241]\n"
"v_cvt_pk_bf16_f32 v250, v242, v243\n"
"v_cvt_pk_bf16_f32 v251, v244, v245\n"
"v_cvt_pk_bf16_f32 v252, v246, v247\n"
"v_cvt_pk_bf16_f32 v253, v248, v249\n"
"s_mov_b32 s63, 18432\n"
"buffer_store_dwordx2 v[250:251], v85, s[8:11], 0 offen\n"
"buffer_store_dwordx2 v[252:253], v85, s[8:11], s63 offen\n"
"s_cmp_eq_u32 s66, 1\n"
"s_cbranch_scc1 .Lg_next_%=\n"
"s_cmp_le_u32 s64, 65\n"
"s_cbranch_scc1 .Lg_prime_%=\n"
"s_or_b32 s63, s57, s60\n"
"s_cbranch_scc1 .Lg_ldone_%=\n"
"s_lshl_b32 s63, s56, 7\n"
"s_add_u32 s63, s63, 35651584\n"
"s_add_u32 s36, s24, s63\n"
"s_addc_u32 s37, s25, 0\n"
"s_mov_b32 s38, 8\n"
"s_mov_b32 s39, 0x20000\n"
"v_mov_b32 v4, s80\n"
"v_mov_b32 v5, s81\n"
"v_lshlrev_b32 v3, 3, v255\n"
"buffer_store_dwordx2 v[4:5], v3, s[36:39], 0 offen sc1\n"
".Lg_ldone_%=:\n"
"s_endpgm\n"
".Lg_compute_%=:\n"
"s_setprio 1\n"
"v_lshrrev_b32 v0, 4, v255\n"
"v_and_b32 v1, 15, v255\n"
"v_bfe_u32 v2, v255, 2, 2\n"
"v_and_b32 v3, 3, v255\n"
"s_lshr_b32 s63, s60, 1\n"
"s_and_b32 s64, s60, 1\n"
"s_lshl_b32 s65, s60, 8\n"
"s_add_u32 s65, s65, 161536\n"
"v_lshl_add_u32 v246, v255, 2, s65\n"
"v_mov_b32 v247, 0\n"
"ds_write_b32 v246, v247\n"
"v_lshlrev_b32 v244, 8, v3\n"
"v_add_u32 v244, 160512, v244\n"
"v_mul_u32_u24 v242, 4352, v0\n"
"v_and_b32 v4, 1, v0\n"
"v_lshl_add_u32 v242, v4, 3, v242\n"
"v_mul_u32_u24 v5, 1088, v2\n"
"v_add_u32 v242, v242, v5\n"
"v_lshl_add_u32 v242, v3, 4, v242\n"
"s_lshl_b32 s65, s63, 8\n"
"s_add_u32 s65, s65, 33792\n"
"v_add_u32 v242, s65, v242\n"
"v_mul_u32_u24 v243, 1056, v3\n"
"v_lshl_add_u32 v243, v2, 7, v243\n"
"v_lshl_add_u32 v243, v0, 4, v243\n"
"s_lshl_b32 s65, s64, 9\n"
"s_add_u32 s65, s65, 103424\n"
"v_add_u32 v243, s65, v243\n"
"v_and_b32 v5, 7, v1\n"
"v_mul_u32_u24 v240, 1056, v5\n"
"v_lshrrev_b32 v5, 3, v1\n"
"v_lshl_add_u32 v240, v5, 8, v240\n"
"v_lshl_add_u32 v240, v0, 4, v240\n"
"v_add_u32 v240, 143616, v240\n"
"v_lshl_add_u32 v5, v4, 2, v2\n"
"v_mul_u32_u24 v5, 1056, v5\n"
"v_lshl_add_u32 v5, v3, 3, v5\n"
"v_lshrrev_b32 v4, 1, v0\n"
"v_lshl_add_u32 v241, v4, 8, v5\n"
"v_lshl_add_u32 v249, v4, 7, v5\n"
"s_lshl_b32 s65, s60, 5\n"
"v_add_u32 v249, s65, v249\n"
"v_lshlrev_b32 v251, 4, v255\n"
"v_lshrrev_b32 v248, 1, v1\n"
"v_mul_u32_u24 v248, 1536, v248\n"
"v_and_b32 v5, 1, v1\n"
"v_lshl_add_u32 v248, v5, 6, v248\n"
"v_lshl_add_u32 v248, v0, 4, v248\n"
"s_mul_i32 s65, s60, 128\n"
"v_add_u32 v248, s65, v248\n"
"v_add_u32 v251, 109824, v251\n"
"s_mul_i32 s65, s60, 12288\n"
"v_add_u32 v254, s65, v251\n"
"v_mov_b32 v247, 1\n"
"v_mov_b32 v108, 0x3f803f80\n"
"v_mov_b32 v109, 0x3f803f80\n"
"v_mov_b32 v110, 0x3f803f80\n"
"v_mov_b32 v111, 0x3f803f80\n"
"s_mov_b32 s76, 0x3e0293ee\n"
"s_mov_b32 s77, 0x3e0293ee\n"
"s_waitcnt lgkmcnt(0)\n"
"s_lshl_b32 s65, s56, 7\n"
"s_add_u32 s65, s65, 35651584\n"
"s_add_u32 s80, s24, s65\n"
"s_addc_u32 s81, s25, 0\n"
"v_mov_b32 v252, 0\n"
"global_load_dwordx2 v[252:253], v252, s[80:81] sc1\n"
"s_lshl_b32 s63, s62, 12\n"
"s_add_u32 s63, s63, 33554432\n"
"s_add_u32 s36, s24, s63\n"
"s_addc_u32 s37, s25, 0\n"
"s_mov_b32 s38, 4096\n"
"s_mov_b32 s39, 0x20000\n"
"s_lshl_b32 s65, s62, 17\n"
"s_add_u32 s40, s14, s65\n"
"s_addc_u32 s41, s15, 0\n"
"s_mov_b32 s42, 131072\n"
"s_mov_b32 s43, 0x20000\n"
"s_add_u32 s44, s16, s65\n"
"s_addc_u32 s45, s17, 0\n"
"s_mov_b32 s46, 131072\n"
"s_mov_b32 s47, 0x20000\n"
"s_mul_i32 s65, s56, 1769472\n"
"s_mul_i32 s66, s57, 12288\n"
"s_add_u32 s65, s65, s66\n"
"s_add_u32 s48, s24, s65\n"
"s_addc_u32 s49, s25, 0\n"
"s_mov_b32 s50, 1585152\n"
"s_mov_b32 s51, 0x20000\n"
"v_mov_b32 v0, 0\n"
"v_mov_b32 v1, 0\n"
"v_mov_b32 v2, 0\n"
"v_mov_b32 v3, 0\n"
"v_mov_b32 v4, 0\n"
"v_mov_b32 v5, 0\n"
"v_mov_b32 v6, 0\n"
"v_mov_b32 v7, 0\n"
"v_mov_b32 v8, 0\n"
"v_mov_b32 v9, 0\n"
"v_mov_b32 v10, 0\n"
"v_mov_b32 v11, 0\n"
"v_mov_b32 v12, 0\n"
"v_mov_b32 v13, 0\n"
"v_mov_b32 v14, 0\n"
"v_mov_b32 v15, 0\n"
"v_mov_b32 v16, 0\n"
"v_mov_b32 v17, 0\n"
"v_mov_b32 v18, 0\n"
"v_mov_b32 v19, 0\n"
"v_mov_b32 v20, 0\n"
"v_mov_b32 v21, 0\n"
"v_mov_b32 v22, 0\n"
"v_mov_b32 v23, 0\n"
"v_mov_b32 v24, 0\n"
"v_mov_b32 v25, 0\n"
"v_mov_b32 v26, 0\n"
"v_mov_b32 v27, 0\n"
"v_mov_b32 v28, 0\n"
"v_mov_b32 v29, 0\n"
"v_mov_b32 v30, 0\n"
"v_mov_b32 v31, 0\n"
"v_mov_b32 v32, 0\n"
"v_mov_b32 v33, 0\n"
"v_mov_b32 v34, 0\n"
"v_mov_b32 v35, 0\n"
"v_mov_b32 v36, 0\n"
"v_mov_b32 v37, 0\n"
"v_mov_b32 v38, 0\n"
"v_mov_b32 v39, 0\n"
"v_mov_b32 v40, 0\n"
"v_mov_b32 v41, 0\n"
"v_mov_b32 v42, 0\n"
"v_mov_b32 v43, 0\n"
"v_mov_b32 v44, 0\n"
"v_mov_b32 v45, 0\n"
"v_mov_b32 v46, 0\n"
"v_mov_b32 v47, 0\n"
"v_mov_b32 v48, 0\n"
"v_mov_b32 v49, 0\n"
"v_mov_b32 v50, 0\n"
"v_mov_b32 v51, 0\n"
"v_mov_b32 v52, 0\n"
"v_mov_b32 v53, 0\n"
"v_mov_b32 v54, 0\n"
"v_mov_b32 v55, 0\n"
"v_mov_b32 v56, 0\n"
"v_mov_b32 v57, 0\n"
"v_mov_b32 v58, 0\n"
"v_mov_b32 v59, 0\n"
"v_mov_b32 v60, 0\n"
"v_mov_b32 v61, 0\n"
"v_mov_b32 v62, 0\n"
"v_mov_b32 v63, 0\n"
"v_mov_b32 v64, 0\n"
"v_mov_b32 v65, 0\n"
"v_mov_b32 v66, 0\n"
"v_mov_b32 v67, 0\n"
"v_mov_b32 v68, 0\n"
"v_mov_b32 v69, 0\n"
"v_mov_b32 v70, 0\n"
"v_mov_b32 v71, 0\n"
"v_mov_b32 v72, 0\n"
"v_mov_b32 v73, 0\n"
"v_mov_b32 v74, 0\n"
"v_mov_b32 v75, 0\n"
"v_mov_b32 v76, 0\n"
"v_mov_b32 v77, 0\n"
"v_mov_b32 v78, 0\n"
"v_mov_b32 v79, 0\n"
"v_mov_b32 v80, 0\n"
"v_mov_b32 v81, 0\n"
"v_mov_b32 v82, 0\n"
"v_mov_b32 v83, 0\n"
"v_mov_b32 v84, 0\n"
"v_mov_b32 v85, 0\n"
"v_mov_b32 v86, 0\n"
"v_mov_b32 v87, 0\n"
"v_mov_b32 v88, 0\n"
"v_mov_b32 v89, 0\n"
"v_mov_b32 v90, 0\n"
"v_mov_b32 v91, 0\n"
"v_mov_b32 v92, 0\n"
"v_mov_b32 v93, 0\n"
"v_mov_b32 v94, 0\n"
"v_mov_b32 v95, 0\n"
"s_mov_b32 s69, 1\n"
"s_mov_b32 s70, 17408\n"
"s_mov_b32 s34, 17408\n"
"s_mov_b32 s71, 1\n"
"s_mov_b32 s72, 12672\n"
"s_mov_b32 s73, 0\n"
"s_waitcnt lgkmcnt(0)\n"
"s_barrier\n"
"ds_read_b128 v[224:227], v243 offset:0\n"
"ds_read_b128 v[228:231], v243 offset:4224\n"
"ds_read_b128 v[232:235], v243 offset:8448\n"
"ds_read_b64_tr_b16 v[192:193], v242 offset:0\n"
"ds_read_b64_tr_b16 v[194:195], v242 offset:512\n"
"ds_read_b64_tr_b16 v[196:197], v242 offset:8\n"
"ds_read_b64_tr_b16 v[198:199], v242 offset:520\n"
"ds_read_b64_tr_b16 v[200:201], v242 offset:64\n"
"ds_read_b64_tr_b16 v[202:203], v242 offset:576\n"
"ds_read_b64_tr_b16 v[204:205], v242 offset:72\n"
"ds_read_b64_tr_b16 v[206:207], v242 offset:584\n"
"ds_read_b64_tr_b16 v[208:209], v242 offset:128\n"
"ds_read_b64_tr_b16 v[210:211], v242 offset:640\n"
"ds_read_b64_tr_b16 v[212:213], v242 offset:136\n"
"ds_read_b64_tr_b16 v[214:215], v242 offset:648\n"
"ds_read_b64_tr_b16 v[216:217], v242 offset:192\n"
"ds_read_b64_tr_b16 v[218:219], v242 offset:704\n"
"ds_read_b64_tr_b16 v[220:221], v242 offset:200\n"
"ds_read_b64_tr_b16 v[222:223], v242 offset:712\n"
"v_add_u32 v242, s70, v242\n"
".Lg_p1loop_%=:\n"
"s_waitcnt lgkmcnt(0)\n"
"s_barrier\n"
"v_mfma_f32_16x16x32_bf16 v[0:3], v[192:195], v[224:227], v[0:3]\n"
"s_add_u32 s69, s69, 1\n"
"ds_read_b128 v[96:99], v243 offset:64\n"
"ds_read_b128 v[100:103], v243 offset:4288\n"
"v_mfma_f32_16x16x32_bf16 v[4:7], v[192:195], v[228:231], v[4:7]\n"
"s_cmp_eq_u32 s69, 4\n"
"ds_read_b128 v[104:107], v243 offset:8512\n"
"v_mfma_f32_16x16x32_bf16 v[8:11], v[192:195], v[232:235], v[8:11]\n"
"s_cselect_b32 s70, -52224, s34\n"
"ds_read_b64_tr_b16 v[160:161], v242 offset:0\n"
"ds_read_b64_tr_b16 v[162:163], v242 offset:512\n"
"v_mfma_f32_16x16x32_bf16 v[12:15], v[196:199], v[224:227], v[12:15]\n"
"s_cselect_b32 s69, 0, s69\n"
"ds_read_b64_tr_b16 v[164:165], v242 offset:8\n"
"v_mfma_f32_16x16x32_bf16 v[16:19], v[196:199], v[228:231], v[16:19]\n"
"s_nop 0\n"
"ds_read_b64_tr_b16 v[166:167], v242 offset:520\n"
"ds_read_b64_tr_b16 v[168:169], v242 offset:64\n"
"v_mfma_f32_16x16x32_bf16 v[20:23], v[196:199], v[232:235], v[20:23]\n"
"s_nop 0\n"
"ds_read_b64_tr_b16 v[170:171], v242 offset:576\n"
"v_mfma_f32_16x16x32_bf16 v[24:27], v[200:203], v[224:227], v[24:27]\n"
"s_nop 0\n"
"ds_read_b64_tr_b16 v[172:173], v242 offset:72\n"
"ds_read_b64_tr_b16 v[174:175], v242 offset:584\n"
"v_mfma_f32_16x16x32_bf16 v[28:31], v[200:203], v[228:231], v[28:31]\n"
"s_nop 0\n"
"ds_read_b64_tr_b16 v[176:177], v242 offset:128\n"
"v_mfma_f32_16x16x32_bf16 v[32:35], v[200:203], v[232:235], v[32:35]\n"
"s_nop 0\n"
"ds_read_b64_tr_b16 v[178:179], v242 offset:640\n"
"ds_read_b64_tr_b16 v[180:181], v242 offset:136\n"
"v_mfma_f32_16x16x32_bf16 v[36:39], v[204:207], v[224:227], v[36:39]\n"
"s_cmp_eq_u32 s73, 8\n"
"ds_read_b64_tr_b16 v[182:183], v242 offset:648\n"
"v_mfma_f32_16x16x32_bf16 v[40:43], v[204:207], v[228:231], v[40:43]\n"
"ds_read_b64_tr_b16 v[184:185], v242 offset:192\n"
"v_mfma_f32_16x16x32_bf16 v[44:47], v[204:207], v[232:235], v[44:47]\n"
"ds_read_b64_tr_b16 v[186:187], v242 offset:704\n"
"v_mfma_f32_16x16x32_bf16 v[48:51], v[208:211], v[224:227], v[48:51]\n"
"ds_read_b64_tr_b16 v[188:189], v242 offset:200\n"
"v_mfma_f32_16x16x32_bf16 v[52:55], v[208:211], v[228:231], v[52:55]\n"
"ds_read_b64_tr_b16 v[190:191], v242 offset:712\n"
"v_mfma_f32_16x16x32_bf16 v[56:59], v[208:211], v[232:235], v[56:59]\n"
"v_mfma_f32_16x16x32_bf16 v[60:63], v[212:215], v[224:227], v[60:63]\n"
"v_mfma_f32_16x16x32_bf16 v[64:67], v[212:215], v[228:231], v[64:67]\n"
"v_mfma_f32_16x16x32_bf16 v[68:71], v[212:215], v[232:235], v[68:71]\n"
"v_mfma_f32_16x16x32_bf16 v[72:75], v[216:219], v[224:227], v[72:75]\n"
"v_mfma_f32_16x16x32_bf16 v[76:79], v[216:219], v[228:231], v[76:79]\n"
"v_mfma_f32_16x16x32_bf16 v[80:83], v[216:219], v[232:235], v[80:83]\n"
"v_add_u32 v242, s70, v242\n"
"v_mfma_f32_16x16x32_bf16 v[84:87], v[220:223], v[224:227], v[84:87]\n"
"v_add_u32 v243, s72, v243\n"
"v_mfma_f32_16x16x32_bf16 v[88:91], v[220:223], v[228:231], v[88:91]\n"
"v_mfma_f32_16x16x32_bf16 v[92:95], v[220:223], v[232:235], v[92:95]\n"
"s_cbranch_scc1 .Lg_p1last_%=\n"
"s_waitcnt lgkmcnt(0)\n"
"s_barrier\n"
"v_mfma_f32_16x16x32_bf16 v[0:3], v[160:163], v[96:99], v[0:3]\n"
"s_add_u32 s69, s69, 1\n"
"ds_read_b128 v[224:227], v243 offset:0\n"
"ds_read_b128 v[228:231], v243 offset:4224\n"
"v_mfma_f32_16x16x32_bf16 v[4:7], v[160:163], v[100:103], v[4:7]\n"
"s_cmp_eq_u32 s69, 4\n"
"ds_read_b128 v[232:235], v243 offset:8448\n"
"v_mfma_f32_16x16x32_bf16 v[8:11], v[160:163], v[104:107], v[8:11]\n"
"s_cselect_b32 s70, -52224, s34\n"
"ds_read_b64_tr_b16 v[192:193], v242 offset:0\n"
"ds_read_b64_tr_b16 v[194:195], v242 offset:512\n"
"v_mfma_f32_16x16x32_bf16 v[12:15], v[164:167], v[96:99], v[12:15]\n"
"s_cselect_b32 s69, 0, s69\n"
"ds_read_b64_tr_b16 v[196:197], v242 offset:8\n"
"v_mfma_f32_16x16x32_bf16 v[16:19], v[164:167], v[100:103], v[16:19]\n"
"s_add_u32 s71, s71, 1\n"
"ds_read_b64_tr_b16 v[198:199], v242 offset:520\n"
"ds_read_b64_tr_b16 v[200:201], v242 offset:64\n"
"v_mfma_f32_16x16x32_bf16 v[20:23], v[164:167], v[104:107], v[20:23]\n"
"s_cmp_eq_u32 s71, 3\n"
"ds_read_b64_tr_b16 v[202:203], v242 offset:576\n"
"v_mfma_f32_16x16x32_bf16 v[24:27], v[168:171], v[96:99], v[24:27]\n"
"s_mov_b32 s72, 12672\n"
"ds_read_b64_tr_b16 v[204:205], v242 offset:72\n"
"ds_read_b64_tr_b16 v[206:207], v242 offset:584\n"
"v_mfma_f32_16x16x32_bf16 v[28:31], v[168:171], v[100:103], v[28:31]\n"
"s_cselect_b32 s72, -25344, s72\n"
"ds_read_b64_tr_b16 v[208:209], v242 offset:128\n"
"v_mfma_f32_16x16x32_bf16 v[32:35], v[168:171], v[104:107], v[32:35]\n"
"s_cselect_b32 s71, 0, s71\n"
"ds_read_b64_tr_b16 v[210:211], v242 offset:640\n"
"ds_read_b64_tr_b16 v[212:213], v242 offset:136\n"
"v_mfma_f32_16x16x32_bf16 v[36:39], v[172:175], v[96:99], v[36:39]\n"
"s_add_u32 s73, s73, 1\n"
"ds_read_b64_tr_b16 v[214:215], v242 offset:648\n"
"v_mfma_f32_16x16x32_bf16 v[40:43], v[172:175], v[100:103], v[40:43]\n"
"ds_read_b64_tr_b16 v[216:217], v242 offset:192\n"
"v_mfma_f32_16x16x32_bf16 v[44:47], v[172:175], v[104:107], v[44:47]\n"
"ds_read_b64_tr_b16 v[218:219], v242 offset:704\n"
"v_mfma_f32_16x16x32_bf16 v[48:51], v[176:179], v[96:99], v[48:51]\n"
"ds_read_b64_tr_b16 v[220:221], v242 offset:200\n"
"v_mfma_f32_16x16x32_bf16 v[52:55], v[176:179], v[100:103], v[52:55]\n"
"ds_read_b64_tr_b16 v[222:223], v242 offset:712\n"
"v_mfma_f32_16x16x32_bf16 v[56:59], v[176:179], v[104:107], v[56:59]\n"
"v_mfma_f32_16x16x32_bf16 v[60:63], v[180:183], v[96:99], v[60:63]\n"
"v_mfma_f32_16x16x32_bf16 v[64:67], v[180:183], v[100:103], v[64:67]\n"
"v_mfma_f32_16x16x32_bf16 v[68:71], v[180:183], v[104:107], v[68:71]\n"
"v_mfma_f32_16x16x32_bf16 v[72:75], v[184:187], v[96:99], v[72:75]\n"
"v_mfma_f32_16x16x32_bf16 v[76:79], v[184:187], v[100:103], v[76:79]\n"
"v_mfma_f32_16x16x32_bf16 v[80:83], v[184:187], v[104:107], v[80:83]\n"
"v_add_u32 v242, s70, v242\n"
"v_mfma_f32_16x16x32_bf16 v[84:87], v[188:191], v[96:99], v[84:87]\n"
"v_mfma_f32_16x16x32_bf16 v[88:91], v[188:191], v[100:103], v[88:91]\n"
"v_mfma_f32_16x16x32_bf16 v[92:95], v[188:191], v[104:107], v[92:95]\n"
"s_branch .Lg_p1loop_%=\n"
".Lg_p1last_%=:\n"
"s_waitcnt lgkmcnt(0)\n"
"s_barrier\n"
"v_mfma_f32_16x16x32_bf16 v[0:3], v[160:163], v[96:99], v[0:3]\n"
"v_mfma_f32_16x16x32_bf16 v[4:7], v[160:163], v[100:103], v[4:7]\n"
"v_mfma_f32_16x16x32_bf16 v[8:11], v[160:163], v[104:107], v[8:11]\n"
"v_mfma_f32_16x16x32_bf16 v[12:15], v[164:167], v[96:99], v[12:15]\n"
"v_mfma_f32_16x16x32_bf16 v[16:19], v[164:167], v[100:103], v[16:19]\n"
"v_mfma_f32_16x16x32_bf16 v[20:23], v[164:167], v[104:107], v[20:23]\n"
"v_mfma_f32_16x16x32_bf16 v[24:27], v[168:171], v[96:99], v[24:27]\n"
"v_mfma_f32_16x16x32_bf16 v[28:31], v[168:171], v[100:103], v[28:31]\n"
"v_mfma_f32_16x16x32_bf16 v[32:35], v[168:171], v[104:107], v[32:35]\n"
"v_mfma_f32_16x16x32_bf16 v[36:39], v[172:175], v[96:99], v[36:39]\n"
"s_mov_b32 s68, 1\n"
"ds_read_b32 v245, v244 offset:0\n"
"v_mfma_f32_16x16x32_bf16 v[40:43], v[172:175], v[100:103], v[40:43]\n"
"v_mfma_f32_16x16x32_bf16 v[44:47], v[172:175], v[104:107], v[44:47]\n"
"v_mul_f32 v0, s76, v0\n"
"v_mul_f32 v1, s76, v1\n"
"v_mfma_f32_16x16x32_bf16 v[48:51], v[176:179], v[96:99], v[48:51]\n"
"v_mul_f32 v2, s76, v2\n"
"v_mul_f32 v3, s76, v3\n"
"v_mfma_f32_16x16x32_bf16 v[52:55], v[176:179], v[100:103], v[52:55]\n"
"v_mul_f32 v12, s76, v12\n"
"v_mul_f32 v13, s76, v13\n"
"v_mfma_f32_16x16x32_bf16 v[56:59], v[176:179], v[104:107], v[56:59]\n"
"v_mul_f32 v14, s76, v14\n"
"v_mul_f32 v15, s76, v15\n"
"v_mfma_f32_16x16x32_bf16 v[60:63], v[180:183], v[96:99], v[60:63]\n"
"v_cvt_pk_bf16_f32 v112, v0, v1\n"
"v_cvt_pk_bf16_f32 v113, v2, v3\n"
"v_mfma_f32_16x16x32_bf16 v[64:67], v[180:183], v[100:103], v[64:67]\n"
"v_cvt_pk_bf16_f32 v114, v12, v13\n"
"v_cvt_pk_bf16_f32 v115, v14, v15\n"
"v_mfma_f32_16x16x32_bf16 v[68:71], v[180:183], v[104:107], v[68:71]\n"
"v_mul_f32 v4, s76, v4\n"
"v_mul_f32 v5, s76, v5\n"
"v_mfma_f32_16x16x32_bf16 v[72:75], v[184:187], v[96:99], v[72:75]\n"
"v_mul_f32 v6, s76, v6\n"
"v_mul_f32 v7, s76, v7\n"
"v_mfma_f32_16x16x32_bf16 v[76:79], v[184:187], v[100:103], v[76:79]\n"
"v_mul_f32 v16, s76, v16\n"
"v_mul_f32 v17, s76, v17\n"
"v_mfma_f32_16x16x32_bf16 v[80:83], v[184:187], v[104:107], v[80:83]\n"
"v_mul_f32 v18, s76, v18\n"
"v_mul_f32 v19, s76, v19\n"
"v_mfma_f32_16x16x32_bf16 v[84:87], v[188:191], v[96:99], v[84:87]\n"
"v_cvt_pk_bf16_f32 v116, v4, v5\n"
"v_cvt_pk_bf16_f32 v117, v6, v7\n"
"v_mfma_f32_16x16x32_bf16 v[88:91], v[188:191], v[100:103], v[88:91]\n"
"v_cvt_pk_bf16_f32 v118, v16, v17\n"
"v_cvt_pk_bf16_f32 v119, v18, v19\n"
"v_mfma_f32_16x16x32_bf16 v[92:95], v[188:191], v[104:107], v[92:95]\n"
"s_waitcnt lgkmcnt(0)\n"
"v_cmp_gt_u32 vcc, s68, v245\n"
"v_pk_mul_f32 v[8:9], v[8:9], s[76:77]\n"
"v_pk_mul_f32 v[10:11], v[10:11], s[76:77]\n"
"v_pk_mul_f32 v[20:21], v[20:21], s[76:77]\n"
"v_pk_mul_f32 v[22:23], v[22:23], s[76:77]\n"
"v_cvt_pk_bf16_f32 v120, v8, v9\n"
"v_cvt_pk_bf16_f32 v121, v10, v11\n"
"v_cvt_pk_bf16_f32 v122, v20, v21\n"
"v_cvt_pk_bf16_f32 v123, v22, v23\n"
"s_cbranch_vccnz .Lg_spin1_%=\n"
".Lg_back2_%=:\n"
"ds_read_b128 v[208:211], v240 offset:8448\n"
"ds_read_b128 v[212:215], v240 offset:8512\n"
"ds_read_b128 v[216:219], v240 offset:8576\n"
"ds_read_b128 v[220:223], v240 offset:8640\n"
"ds_read_b128 v[224:227], v240 offset:8960\n"
"ds_read_b128 v[228:231], v240 offset:9024\n"
"ds_read_b128 v[232:235], v240 offset:9088\n"
"ds_read_b128 v[236:239], v240 offset:9152\n"
"v_pk_mul_f32 v[24:25], v[24:25], s[76:77]\n"
"v_pk_mul_f32 v[26:27], v[26:27], s[76:77]\n"
"v_pk_mul_f32 v[36:37], v[36:37], s[76:77]\n"
"v_pk_mul_f32 v[38:39], v[38:39], s[76:77]\n"
"v_cvt_pk_bf16_f32 v124, v24, v25\n"
"v_cvt_pk_bf16_f32 v125, v26, v27\n"
"v_cvt_pk_bf16_f32 v126, v36, v37\n"
"v_cvt_pk_bf16_f32 v127, v38, v39\n"
"v_pk_mul_f32 v[28:29], v[28:29], s[76:77]\n"
"v_pk_mul_f32 v[30:31], v[30:31], s[76:77]\n"
"v_pk_mul_f32 v[40:41], v[40:41], s[76:77]\n"
"v_pk_mul_f32 v[42:43], v[42:43], s[76:77]\n"
"v_cvt_pk_bf16_f32 v128, v28, v29\n"
"v_cvt_pk_bf16_f32 v129, v30, v31\n"
"v_cvt_pk_bf16_f32 v130, v40, v41\n"
"v_cvt_pk_bf16_f32 v131, v42, v43\n"
"v_pk_mul_f32 v[32:33], v[32:33], s[76:77]\n"
"v_pk_mul_f32 v[34:35], v[34:35], s[76:77]\n"
"v_pk_mul_f32 v[44:45], v[44:45], s[76:77]\n"
"v_pk_mul_f32 v[46:47], v[46:47], s[76:77]\n"
"v_cvt_pk_bf16_f32 v132, v32, v33\n"
"v_cvt_pk_bf16_f32 v133, v34, v35\n"
"v_cvt_pk_bf16_f32 v134, v44, v45\n"
"v_cvt_pk_bf16_f32 v135, v46, v47\n"
"v_pk_mul_f32 v[48:49], v[48:49], s[76:77]\n"
"v_pk_mul_f32 v[50:51], v[50:51], s[76:77]\n"
"v_pk_mul_f32 v[60:61], v[60:61], s[76:77]\n"
"v_pk_mul_f32 v[62:63], v[62:63], s[76:77]\n"
"v_cvt_pk_bf16_f32 v136, v48, v49\n"
"v_cvt_pk_bf16_f32 v137, v50, v51\n"
"v_cvt_pk_bf16_f32 v138, v60, v61\n"
"v_cvt_pk_bf16_f32 v139, v62, v63\n"
"v_pk_mul_f32 v[52:53], v[52:53], s[76:77]\n"
"v_pk_mul_f32 v[54:55], v[54:55], s[76:77]\n"
"v_pk_mul_f32 v[64:65], v[64:65], s[76:77]\n"
"v_pk_mul_f32 v[66:67], v[66:67], s[76:77]\n"
"v_cvt_pk_bf16_f32 v140, v52, v53\n"
"v_cvt_pk_bf16_f32 v141, v54, v55\n"
"v_cvt_pk_bf16_f32 v142, v64, v65\n"
"v_cvt_pk_bf16_f32 v143, v66, v67\n"
"v_pk_mul_f32 v[56:57], v[56:57], s[76:77]\n"
"v_pk_mul_f32 v[58:59], v[58:59], s[76:77]\n"
"v_pk_mul_f32 v[68:69], v[68:69], s[76:77]\n"
"v_pk_mul_f32 v[70:71], v[70:71], s[76:77]\n"
"v_cvt_pk_bf16_f32 v144, v56, v57\n"
"v_cvt_pk_bf16_f32 v145, v58, v59\n"
"v_cvt_pk_bf16_f32 v146, v68, v69\n"
"v_cvt_pk_bf16_f32 v147, v70, v71\n"
"v_pk_mul_f32 v[72:73], v[72:73], s[76:77]\n"
"v_pk_mul_f32 v[74:75], v[74:75], s[76:77]\n"
"v_pk_mul_f32 v[84:85], v[84:85], s[76:77]\n"
"v_pk_mul_f32 v[86:87], v[86:87], s[76:77]\n"
"v_cvt_pk_bf16_f32 v148, v72, v73\n"
"v_cvt_pk_bf16_f32 v149, v74, v75\n"
"v_cvt_pk_bf16_f32 v150, v84, v85\n"
"v_cvt_pk_bf16_f32 v151, v86, v87\n"
"v_pk_mul_f32 v[76:77], v[76:77], s[76:77]\n"
"v_pk_mul_f32 v[78:79], v[78:79], s[76:77]\n"
"v_pk_mul_f32 v[88:89], v[88:89], s[76:77]\n"
"v_pk_mul_f32 v[90:91], v[90:91], s[76:77]\n"
"v_cvt_pk_bf16_f32 v152, v76, v77\n"
"v_cvt_pk_bf16_f32 v153, v78, v79\n"
"v_cvt_pk_bf16_f32 v154, v88, v89\n"
"v_cvt_pk_bf16_f32 v155, v90, v91\n"
"v_pk_mul_f32 v[80:81], v[80:81], s[76:77]\n"
"v_pk_mul_f32 v[82:83], v[82:83], s[76:77]\n"
"v_pk_mul_f32 v[92:93], v[92:93], s[76:77]\n"
"v_pk_mul_f32 v[94:95], v[94:95], s[76:77]\n"
"v_cvt_pk_bf16_f32 v156, v80, v81\n"
"v_cvt_pk_bf16_f32 v157, v82, v83\n"
"v_cvt_pk_bf16_f32 v158, v92, v93\n"
"v_cvt_pk_bf16_f32 v159, v94, v95\n"
"v_mov_b32 v196, 0\n"
"v_mov_b32 v197, 0\n"
"v_mov_b32 v198, 0\n"
"v_mov_b32 v199, 0\n"
"s_mov_b32 s68, 2\n"
"v_add_u32 v240, 4294823680, v240\n"
"s_mov_b32 s34, 16896\n"
"s_waitcnt lgkmcnt(4)\n"
"v_mfma_f32_16x16x32_bf16 v[172:175], v[208:211], v[112:115], 0\n"
"v_mfma_f32_16x16x32_bf16 v[172:175], v[212:215], v[124:127], v[172:175]\n"
"v_mfma_f32_16x16x32_bf16 v[172:175], v[216:219], v[136:139], v[172:175]\n"
"v_mfma_f32_16x16x32_bf16 v[172:175], v[220:223], v[148:151], v[172:175]\n"
"v_mfma_f32_16x16x32_bf16 v[176:179], v[208:211], v[116:119], 0\n"
"v_mfma_f32_16x16x32_bf16 v[176:179], v[212:215], v[128:131], v[176:179]\n"
"v_mfma_f32_16x16x32_bf16 v[176:179], v[216:219], v[140:143], v[176:179]\n"
"v_mfma_f32_16x16x32_bf16 v[176:179], v[220:223], v[152:155], v[176:179]\n"
"v_mfma_f32_16x16x32_bf16 v[180:183], v[208:211], v[120:123], 0\n"
"v_mfma_f32_16x16x32_bf16 v[180:183], v[212:215], v[132:135], v[180:183]\n"
"v_mfma_f32_16x16x32_bf16 v[180:183], v[216:219], v[144:147], v[180:183]\n"
"v_mfma_f32_16x16x32_bf16 v[180:183], v[220:223], v[156:159], v[180:183]\n"
"v_mfma_f32_16x16x32_bf16 v[0:3], v[196:199], v[196:199], 0\n"
"ds_read_b32 v245, v244 offset:0\n"
"v_mfma_f32_16x16x32_bf16 v[4:7], v[196:199], v[196:199], 0\n"
"v_mfma_f32_16x16x32_bf16 v[8:11], v[196:199], v[196:199], 0\n"
"v_mfma_f32_16x16x32_bf16 v[12:15], v[196:199], v[196:199], 0\n"
"v_max3_f32 v184, v172, v173, v174\n"
"v_max3_f32 v188, v176, v177, v178\n"
"v_mfma_f32_16x16x32_bf16 v[16:19], v[196:199], v[196:199], 0\n"
"v_max3_f32 v192, v180, v181, v182\n"
"v_max_f32 v184, v184, v175\n"
"v_mfma_f32_16x16x32_bf16 v[20:23], v[196:199], v[196:199], 0\n"
"v_max_f32 v188, v188, v179\n"
"v_max_f32 v192, v192, v183\n"
"v_mfma_f32_16x16x32_bf16 v[24:27], v[196:199], v[196:199], 0\n"
"v_mov_b32 v185, v184\n"
"v_mov_b32 v189, v188\n"
"v_mfma_f32_16x16x32_bf16 v[28:31], v[196:199], v[196:199], 0\n"
"v_mov_b32 v193, v192\n"
"v_permlane32_swap_b32 v184, v185\n"
"v_mfma_f32_16x16x32_bf16 v[32:35], v[196:199], v[196:199], 0\n"
"v_permlane32_swap_b32 v188, v189\n"
"v_permlane32_swap_b32 v192, v193\n"
"v_mfma_f32_16x16x32_bf16 v[36:39], v[196:199], v[196:199], 0\n"
"v_max_f32 v184, v184, v185\n"
"v_max_f32 v188, v188, v189\n"
"v_mfma_f32_16x16x32_bf16 v[40:43], v[196:199], v[196:199], 0\n"
"v_max_f32 v192, v192, v193\n"
"v_mov_b32 v185, v184\n"
"v_mfma_f32_16x16x32_bf16 v[44:47], v[196:199], v[196:199], 0\n"
"v_mov_b32 v189, v188\n"
"v_mov_b32 v193, v192\n"
"v_mfma_f32_16x16x32_bf16 v[48:51], v[196:199], v[196:199], 0\n"
"v_permlane16_swap_b32 v184, v185\n"
"v_permlane16_swap_b32 v188, v189\n"
"v_mfma_f32_16x16x32_bf16 v[52:55], v[196:199], v[196:199], 0\n"
"v_permlane16_swap_b32 v192, v193\n"
"v_max_f32 v184, v184, v185\n"
"v_mfma_f32_16x16x32_bf16 v[56:59], v[196:199], v[196:199], 0\n"
"v_max_f32 v188, v188, v189\n"
"v_max_f32 v192, v192, v193\n"
"v_mfma_f32_16x16x32_bf16 v[60:63], v[196:199], v[196:199], 0\n"
"v_sub_f32 v160, 0, v184\n"
"v_sub_f32 v161, 0, v184\n"
"v_mfma_f32_16x16x32_bf16 v[64:67], v[196:199], v[196:199], 0\n"
"v_sub_f32 v162, 0, v184\n"
"v_sub_f32 v163, 0, v184\n"
"v_mfma_f32_16x16x32_bf16 v[68:71], v[196:199], v[196:199], 0\n"
"v_sub_f32 v164, 0, v188\n"
"v_sub_f32 v165, 0, v188\n"
"v_mfma_f32_16x16x32_bf16 v[72:75], v[196:199], v[196:199], 0\n"
"v_sub_f32 v166, 0, v188\n"
"v_sub_f32 v167, 0, v188\n"
"v_mfma_f32_16x16x32_bf16 v[76:79], v[196:199], v[196:199], 0\n"
"v_sub_f32 v168, 0, v192\n"
"v_sub_f32 v169, 0, v192\n"
"v_mfma_f32_16x16x32_bf16 v[80:83], v[196:199], v[196:199], 0\n"
"v_sub_f32 v170, 0, v192\n"
"v_sub_f32 v171, 0, v192\n"
"v_mfma_f32_16x16x32_bf16 v[84:87], v[196:199], v[196:199], 0\n"
"v_sub_f32 v172, v172, v184\n"
"v_sub_f32 v173, v173, v184\n"
"v_mfma_f32_16x16x32_bf16 v[88:91], v[196:199], v[196:199], 0\n"
"v_sub_f32 v174, v174, v184\n"
"v_sub_f32 v175, v175, v184\n"
"v_mfma_f32_16x16x32_bf16 v[92:95], v[196:199], v[196:199], 0\n"
"v_sub_f32 v176, v176, v188\n"
"v_sub_f32 v177, v177, v188\n"
"v_mfma_f32_16x16x32_bf16 v[96:99], v[196:199], v[196:199], 0\n"
"v_sub_f32 v178, v178, v188\n"
"v_sub_f32 v179, v179, v188\n"
"v_mfma_f32_16x16x32_bf16 v[100:103], v[196:199], v[196:199], 0\n"
"v_sub_f32 v180, v180, v192\n"
"v_sub_f32 v181, v181, v192\n"
"v_mfma_f32_16x16x32_bf16 v[104:107], v[196:199], v[196:199], 0\n"
"v_sub_f32 v182, v182, v192\n"
"v_sub_f32 v183, v183, v192\n"
"s_waitcnt lgkmcnt(0)\n"
"v_cmp_gt_u32 vcc, s68, v245\n"
"v_mfma_f32_16x16x32_bf16 v[184:187], v[224:227], v[112:115], v[160:163]\n"
"v_exp_f32 v172, v172\n"
"v_mfma_f32_16x16x32_bf16 v[184:187], v[228:231], v[124:127], v[184:187]\n"
"s_cbranch_vccnz .Lg_spin3_%=\n"
".Lg_back4_%=:\n"
"ds_read_b128 v[208:211], v240 offset:8448\n"
"ds_read_b128 v[212:215], v240 offset:8512\n"
"v_mfma_f32_16x16x32_bf16 v[184:187], v[232:235], v[136:139], v[184:187]\n"
"v_exp_f32 v173, v173\n"
"v_mfma_f32_16x16x32_bf16 v[184:187], v[236:239], v[148:151], v[184:187]\n"
"ds_read_b128 v[216:219], v240 offset:8576\n"
"ds_read_b128 v[220:223], v240 offset:8640\n"
"v_mfma_f32_16x16x32_bf16 v[188:191], v[224:227], v[116:119], v[164:167]\n"
"v_exp_f32 v174, v174\n"
"v_mfma_f32_16x16x32_bf16 v[188:191], v[228:231], v[128:131], v[188:191]\n"
"v_exp_f32 v175, v175\n"
"v_mfma_f32_16x16x32_bf16 v[188:191], v[232:235], v[140:143], v[188:191]\n"
"v_exp_f32 v176, v176\n"
"v_mfma_f32_16x16x32_bf16 v[188:191], v[236:239], v[152:155], v[188:191]\n"
"v_exp_f32 v177, v177\n"
"v_mfma_f32_16x16x32_bf16 v[192:195], v[224:227], v[120:123], v[168:171]\n"
"v_exp_f32 v178, v178\n"
"v_mfma_f32_16x16x32_bf16 v[192:195], v[228:231], v[132:135], v[192:195]\n"
"v_exp_f32 v179, v179\n"
"v_mfma_f32_16x16x32_bf16 v[192:195], v[232:235], v[144:147], v[192:195]\n"
"v_exp_f32 v180, v180\n"
"v_mfma_f32_16x16x32_bf16 v[192:195], v[236:239], v[156:159], v[192:195]\n"
"v_exp_f32 v181, v181\n"
"ds_read_b64_tr_b16 v[224:225], v241 offset:0\n"
"ds_read_b64_tr_b16 v[226:227], v241 offset:512\n"
"ds_read_b64_tr_b16 v[228:229], v241 offset:32\n"
"ds_read_b64_tr_b16 v[230:231], v241 offset:544\n"
"v_exp_f32 v182, v182\n"
"v_exp_f32 v183, v183\n"
"v_exp_f32 v184, v184\n"
"v_exp_f32 v185, v185\n"
"v_exp_f32 v186, v186\n"
"v_exp_f32 v187, v187\n"
"v_exp_f32 v188, v188\n"
"v_exp_f32 v189, v189\n"
"v_exp_f32 v190, v190\n"
"v_exp_f32 v191, v191\n"
"v_exp_f32 v192, v192\n"
"v_cvt_pk_bf16_f32 v196, v172, v173\n"
"v_cvt_pk_bf16_f32 v197, v174, v175\n"
"s_waitcnt lgkmcnt(4)\n"
".Lg_p2loop_%=:\n"
"v_mfma_f32_16x16x32_bf16 v[172:175], v[208:211], v[112:115], v[160:163]\n"
"v_exp_f32 v193, v193\n"
"v_mfma_f32_16x16x32_bf16 v[172:175], v[212:215], v[124:127], v[172:175]\n"
"ds_read_b64_tr_b16 v[232:233], v241 offset:64\n"
"v_cvt_pk_bf16_f32 v200, v176, v177\n"
"v_mfma_f32_16x16x32_bf16 v[172:175], v[216:219], v[136:139], v[172:175]\n"
"v_exp_f32 v194, v194\n"
"v_mfma_f32_16x16x32_bf16 v[172:175], v[220:223], v[148:151], v[172:175]\n"
"ds_read_b64_tr_b16 v[234:235], v241 offset:576\n"
"v_cvt_pk_bf16_f32 v201, v178, v179\n"
"v_mfma_f32_16x16x32_bf16 v[176:179], v[208:211], v[116:119], v[164:167]\n"
"v_exp_f32 v195, v195\n"
"v_mfma_f32_16x16x32_bf16 v[176:179], v[212:215], v[128:131], v[176:179]\n"
"ds_read_b64_tr_b16 v[236:237], v241 offset:96\n"
"v_cvt_pk_bf16_f32 v204, v180, v181\n"
"v_mfma_f32_16x16x32_bf16 v[176:179], v[216:219], v[140:143], v[176:179]\n"
"ds_read_b64_tr_b16 v[238:239], v241 offset:608\n"
"v_cvt_pk_bf16_f32 v205, v182, v183\n"
"v_mfma_f32_16x16x32_bf16 v[176:179], v[220:223], v[152:155], v[176:179]\n"
"v_exp_f32 v172, v172\n"
"v_mfma_f32_16x16x32_bf16 v[180:183], v[208:211], v[120:123], v[168:171]\n"
"v_cvt_pk_bf16_f32 v198, v184, v185\n"
"v_cvt_pk_bf16_f32 v199, v186, v187\n"
"v_mfma_f32_16x16x32_bf16 v[180:183], v[212:215], v[132:135], v[180:183]\n"
"v_exp_f32 v173, v173\n"
"v_mfma_f32_16x16x32_bf16 v[180:183], v[216:219], v[144:147], v[180:183]\n"
"v_cvt_pk_bf16_f32 v202, v188, v189\n"
"v_cvt_pk_bf16_f32 v203, v190, v191\n"
"v_mfma_f32_16x16x32_bf16 v[180:183], v[220:223], v[156:159], v[180:183]\n"
"s_waitcnt lgkmcnt(4)\n"
"v_exp_f32 v174, v174\n"
"v_mfma_f32_16x16x32_bf16 v[0:3], v[224:227], v[196:199], v[0:3]\n"
"v_cvt_pk_bf16_f32 v206, v192, v193\n"
"v_cvt_pk_bf16_f32 v207, v194, v195\n"
"v_mfma_f32_16x16x32_bf16 v[4:7], v[224:227], v[200:203], v[4:7]\n"
"v_exp_f32 v175, v175\n"
"v_mfma_f32_16x16x32_bf16 v[8:11], v[224:227], v[204:207], v[8:11]\n"
"ds_read_b128 v[208:211], v240 offset:8960\n"
"ds_read_b128 v[212:215], v240 offset:9024\n"
"v_mfma_f32_16x16x32_bf16 v[96:99], v[108:111], v[196:199], v[96:99]\n"
"v_exp_f32 v176, v176\n"
"v_mfma_f32_16x16x32_bf16 v[100:103], v[108:111], v[200:203], v[100:103]\n"
"ds_read_b128 v[216:219], v240 offset:9088\n"
"ds_read_b128 v[220:223], v240 offset:9152\n"
"v_mfma_f32_16x16x32_bf16 v[12:15], v[228:231], v[196:199], v[12:15]\n"
"v_exp_f32 v177, v177\n"
"v_mfma_f32_16x16x32_bf16 v[16:19], v[228:231], v[200:203], v[16:19]\n"
"ds_read_b64_tr_b16 v[224:225], v241 offset:128\n"
"ds_read_b64_tr_b16 v[226:227], v241 offset:640\n"
"v_mfma_f32_16x16x32_bf16 v[20:23], v[228:231], v[204:207], v[20:23]\n"
"s_waitcnt lgkmcnt(8)\n"
"v_exp_f32 v178, v178\n"
"v_mfma_f32_16x16x32_bf16 v[24:27], v[232:235], v[196:199], v[24:27]\n"
"ds_read_b64_tr_b16 v[228:229], v241 offset:160\n"
"ds_read_b64_tr_b16 v[230:231], v241 offset:672\n"
"v_mfma_f32_16x16x32_bf16 v[28:31], v[232:235], v[200:203], v[28:31]\n"
"s_waitcnt lgkmcnt(8)\n"
"v_exp_f32 v179, v179\n"
"v_mfma_f32_16x16x32_bf16 v[32:35], v[232:235], v[204:207], v[32:35]\n"
"ds_read_b64_tr_b16 v[232:233], v241 offset:192\n"
"ds_read_b64_tr_b16 v[234:235], v241 offset:704\n"
"v_mfma_f32_16x16x32_bf16 v[36:39], v[236:239], v[196:199], v[36:39]\n"
"s_add_u32 s68, s68, 1\n"
"v_exp_f32 v180, v180\n"
"v_mfma_f32_16x16x32_bf16 v[40:43], v[236:239], v[200:203], v[40:43]\n"
"s_cmp_eq_u32 s68, 11\n"
"s_cselect_b32 s70, -135168, s34\n"
"v_mfma_f32_16x16x32_bf16 v[44:47], v[236:239], v[204:207], v[44:47]\n"
"s_waitcnt lgkmcnt(6)\n"
"v_exp_f32 v181, v181\n"
"v_mfma_f32_16x16x32_bf16 v[184:187], v[208:211], v[112:115], v[160:163]\n"
"ds_read_b64_tr_b16 v[236:237], v241 offset:224\n"
"ds_read_b64_tr_b16 v[238:239], v241 offset:736\n"
"v_mfma_f32_16x16x32_bf16 v[184:187], v[212:215], v[124:127], v[184:187]\n"
"v_exp_f32 v182, v182\n"
"v_mfma_f32_16x16x32_bf16 v[184:187], v[216:219], v[136:139], v[184:187]\n"
"ds_read_b32 v245, v244 offset:0\n"
"v_add_u32 v240, s70, v240\n"
"v_mfma_f32_16x16x32_bf16 v[184:187], v[220:223], v[148:151], v[184:187]\n"
"v_exp_f32 v183, v183\n"
"v_mfma_f32_16x16x32_bf16 v[188:191], v[208:211], v[116:119], v[164:167]\n"
"v_add_u32 v241, s70, v241\n"
"v_mfma_f32_16x16x32_bf16 v[188:191], v[212:215], v[128:131], v[188:191]\n"
"s_nop 0\n"
"v_mfma_f32_16x16x32_bf16 v[188:191], v[216:219], v[140:143], v[188:191]\n"
"s_nop 0\n"
"v_mfma_f32_16x16x32_bf16 v[188:191], v[220:223], v[152:155], v[188:191]\n"
"v_exp_f32 v184, v184\n"
"v_mfma_f32_16x16x32_bf16 v[192:195], v[208:211], v[120:123], v[168:171]\n"
"s_waitcnt lgkmcnt(0)\n"
"v_cmp_gt_u32 vcc, s68, v245\n"
"v_mfma_f32_16x16x32_bf16 v[192:195], v[212:215], v[132:135], v[192:195]\n"
"v_exp_f32 v185, v185\n"
"v_mfma_f32_16x16x32_bf16 v[192:195], v[216:219], v[144:147], v[192:195]\n"
"ds_add_u32 v246, v247\n"
"v_mfma_f32_16x16x32_bf16 v[192:195], v[220:223], v[156:159], v[192:195]\n"
"v_exp_f32 v186, v186\n"
"v_mfma_f32_16x16x32_bf16 v[48:51], v[224:227], v[196:199], v[48:51]\n"
"s_cbranch_vccnz .Lg_spin5_%=\n"
".Lg_back6_%=:\n"
"ds_read_b128 v[208:211], v240 offset:8448\n"
"ds_read_b128 v[212:215], v240 offset:8512\n"
"v_mfma_f32_16x16x32_bf16 v[52:55], v[224:227], v[200:203], v[52:55]\n"
"v_exp_f32 v187, v187\n"
"v_mfma_f32_16x16x32_bf16 v[56:59], v[224:227], v[204:207], v[56:59]\n"
"ds_read_b128 v[216:219], v240 offset:8576\n"
"ds_read_b128 v[220:223], v240 offset:8640\n"
"v_mfma_f32_16x16x32_bf16 v[104:107], v[108:111], v[204:207], v[104:107]\n"
"v_exp_f32 v188, v188\n"
"v_mfma_f32_16x16x32_bf16 v[60:63], v[228:231], v[196:199], v[60:63]\n"
"ds_read_b64_tr_b16 v[224:225], v241 offset:0\n"
"ds_read_b64_tr_b16 v[226:227], v241 offset:512\n"
"v_mfma_f32_16x16x32_bf16 v[64:67], v[228:231], v[200:203], v[64:67]\n"
"v_exp_f32 v189, v189\n"
"v_mfma_f32_16x16x32_bf16 v[68:71], v[228:231], v[204:207], v[68:71]\n"
"ds_read_b64_tr_b16 v[228:229], v241 offset:32\n"
"ds_read_b64_tr_b16 v[230:231], v241 offset:544\n"
"v_mfma_f32_16x16x32_bf16 v[72:75], v[232:235], v[196:199], v[72:75]\n"
"v_exp_f32 v190, v190\n"
"v_mfma_f32_16x16x32_bf16 v[76:79], v[232:235], v[200:203], v[76:79]\n"
"s_cmp_lt_u32 s68, 17\n"
"v_mfma_f32_16x16x32_bf16 v[80:83], v[232:235], v[204:207], v[80:83]\n"
"v_exp_f32 v191, v191\n"
"v_mfma_f32_16x16x32_bf16 v[84:87], v[236:239], v[196:199], v[84:87]\n"
"v_cvt_pk_bf16_f32 v196, v172, v173\n"
"v_cvt_pk_bf16_f32 v197, v174, v175\n"
"v_mfma_f32_16x16x32_bf16 v[88:91], v[236:239], v[200:203], v[88:91]\n"
"s_waitcnt lgkmcnt(4)\n"
"v_exp_f32 v192, v192\n"
"v_mfma_f32_16x16x32_bf16 v[92:95], v[236:239], v[204:207], v[92:95]\n"
"s_cbranch_scc1 .Lg_p2loop_%=\n"
"ds_read_b64_tr_b16 v[232:233], v241 offset:64\n"
"ds_read_b64_tr_b16 v[234:235], v241 offset:576\n"
"ds_read_b64_tr_b16 v[236:237], v241 offset:96\n"
"ds_read_b64_tr_b16 v[238:239], v241 offset:608\n"
"ds_read_b64_tr_b16 v[208:209], v241 offset:128\n"
"ds_read_b64_tr_b16 v[210:211], v241 offset:640\n"
"ds_read_b64_tr_b16 v[212:213], v241 offset:160\n"
"ds_read_b64_tr_b16 v[214:215], v241 offset:672\n"
"ds_read_b64_tr_b16 v[216:217], v241 offset:192\n"
"ds_read_b64_tr_b16 v[218:219], v241 offset:704\n"
"ds_read_b64_tr_b16 v[220:221], v241 offset:224\n"
"ds_read_b64_tr_b16 v[222:223], v241 offset:736\n"
"ds_read_b32 v245, v244 offset:1024\n"
"v_exp_f32 v193, v193\n"
"v_exp_f32 v194, v194\n"
"v_exp_f32 v195, v195\n"
"v_cvt_pk_bf16_f32 v200, v176, v177\n"
"v_cvt_pk_bf16_f32 v201, v178, v179\n"
"v_cvt_pk_bf16_f32 v204, v180, v181\n"
"v_cvt_pk_bf16_f32 v205, v182, v183\n"
"v_cvt_pk_bf16_f32 v198, v184, v185\n"
"v_cvt_pk_bf16_f32 v199, v186, v187\n"
"v_cvt_pk_bf16_f32 v202, v188, v189\n"
"v_cvt_pk_bf16_f32 v203, v190, v191\n"
"v_cvt_pk_bf16_f32 v206, v192, v193\n"
"v_cvt_pk_bf16_f32 v207, v194, v195\n"
"v_mfma_f32_16x16x32_bf16 v[96:99], v[108:111], v[196:199], v[96:99]\n"
"v_mfma_f32_16x16x32_bf16 v[100:103], v[108:111], v[200:203], v[100:103]\n"
"v_mfma_f32_16x16x32_bf16 v[104:107], v[108:111], v[204:207], v[104:107]\n"
"s_waitcnt lgkmcnt(9)\n"
"v_mfma_f32_16x16x32_bf16 v[0:3], v[224:227], v[196:199], v[0:3]\n"
"s_waitcnt lgkmcnt(0)\n"
"v_cmp_gt_u32 vcc, 9, v245\n"
"v_mfma_f32_16x16x32_bf16 v[12:15], v[228:231], v[196:199], v[12:15]\n"
"ds_add_u32 v246, v247\n"
"s_nop 1\n"
"v_max3_f32 v180, v96, v100, v104\n"
"v_mfma_f32_16x16x32_bf16 v[24:27], v[232:235], v[196:199], v[24:27]\n"
"s_cbranch_vccnz .Lg_spin7_%=\n"
".Lg_back8_%=:\n"
"v_rcp_f32 v172, v96\n"
"v_mfma_f32_16x16x32_bf16 v[36:39], v[236:239], v[196:199], v[36:39]\n"
"v_rcp_f32 v174, v100\n"
"v_mfma_f32_16x16x32_bf16 v[48:51], v[208:211], v[196:199], v[48:51]\n"
"v_rcp_f32 v176, v104\n"
"v_mfma_f32_16x16x32_bf16 v[60:63], v[212:215], v[196:199], v[60:63]\n"
"s_cmp_ge_u32 s56, 9\n"
"ds_read_b32 v245, v244 offset:0\n"
"v_mul_f32 v0, v172, v0\n"
"v_mfma_f32_16x16x32_bf16 v[72:75], v[216:219], v[196:199], v[72:75]\n"
"s_cselect_b32 s75, 9, 0\n"
"v_mul_f32 v1, v172, v1\n"
"v_mul_f32 v2, v172, v2\n"
"v_mfma_f32_16x16x32_bf16 v[84:87], v[220:223], v[196:199], v[84:87]\n"
"s_sub_u32 s75, s56, s75\n"
"v_mul_f32 v3, v172, v3\n"
"v_mul_f32 v12, v172, v12\n"
"v_mfma_f32_16x16x32_bf16 v[4:7], v[224:227], v[200:203], v[4:7]\n"
"s_mul_i32 s74, s75, 196608\n"
"v_mul_f32 v13, v172, v13\n"
"v_mul_f32 v14, v172, v14\n"
"v_mfma_f32_16x16x32_bf16 v[16:19], v[228:231], v[200:203], v[16:19]\n"
"s_mov_b32 s69, 1\n"
"v_mul_f32 v15, v172, v15\n"
"v_cvt_pk_bf16_f32 v160, v0, v1\n"
"v_mfma_f32_16x16x32_bf16 v[28:31], v[232:235], v[200:203], v[28:31]\n"
"s_mov_b32 s70, 33792\n"
"v_cvt_pk_bf16_f32 v161, v2, v3\n"
"v_cvt_pk_bf16_f32 v162, v12, v13\n"
"v_mfma_f32_16x16x32_bf16 v[40:43], v[236:239], v[200:203], v[40:43]\n"
"s_mov_b32 s34, 33792\n"
"v_cvt_pk_bf16_f32 v163, v14, v15\n"
"v_mfma_f32_16x16x32_bf16 v[52:55], v[208:211], v[200:203], v[52:55]\n"
"s_mov_b32 s73, 1\n"
"ds_write_b128 v254, v[160:163] offset:0\n"
"v_mul_f32 v24, v172, v24\n"
"v_mfma_f32_16x16x32_bf16 v[64:67], v[212:215], v[200:203], v[64:67]\n"
"s_mov_b32 s68, 18\n"
"v_mul_f32 v25, v172, v25\n"
"v_mul_f32 v26, v172, v26\n"
"v_mfma_f32_16x16x32_bf16 v[76:79], v[216:219], v[200:203], v[76:79]\n"
"v_mul_f32 v27, v172, v27\n"
"v_mul_f32 v36, v172, v36\n"
"v_mfma_f32_16x16x32_bf16 v[88:91], v[220:223], v[200:203], v[88:91]\n"
"v_mul_f32 v37, v172, v37\n"
"v_mul_f32 v38, v172, v38\n"
"v_mfma_f32_16x16x32_bf16 v[8:11], v[224:227], v[204:207], v[8:11]\n"
"v_mul_f32 v39, v172, v39\n"
"v_cvt_pk_bf16_f32 v164, v24, v25\n"
"v_mfma_f32_16x16x32_bf16 v[20:23], v[228:231], v[204:207], v[20:23]\n"
"v_cvt_pk_bf16_f32 v165, v26, v27\n"
"v_cvt_pk_bf16_f32 v166, v36, v37\n"
"v_mfma_f32_16x16x32_bf16 v[32:35], v[232:235], v[204:207], v[32:35]\n"
"v_cvt_pk_bf16_f32 v167, v38, v39\n"
"v_mfma_f32_16x16x32_bf16 v[44:47], v[236:239], v[204:207], v[44:47]\n"
"ds_write_b128 v254, v[164:167] offset:3072\n"
"v_mul_f32 v48, v172, v48\n"
"v_mfma_f32_16x16x32_bf16 v[56:59], v[208:211], v[204:207], v[56:59]\n"
"v_mul_f32 v49, v172, v49\n"
"v_mul_f32 v50, v172, v50\n"
"v_mfma_f32_16x16x32_bf16 v[68:71], v[212:215], v[204:207], v[68:71]\n"
"v_mul_f32 v51, v172, v51\n"
"v_mul_f32 v60, v172, v60\n"
"v_mfma_f32_16x16x32_bf16 v[80:83], v[216:219], v[204:207], v[80:83]\n"
"v_mul_f32 v61, v172, v61\n"
"v_mul_f32 v62, v172, v62\n"
"v_mfma_f32_16x16x32_bf16 v[92:95], v[220:223], v[204:207], v[92:95]\n"
"v_cmp_ngt_f32 vcc, 0x5d800000, v180\n"
"v_mul_f32 v63, v172, v63\n"
"v_cvt_pk_bf16_f32 v168, v48, v49\n"
"v_cvt_pk_bf16_f32 v169, v50, v51\n"
"v_cvt_pk_bf16_f32 v170, v60, v61\n"
"v_cvt_pk_bf16_f32 v171, v62, v63\n"
"ds_write_b128 v254, v[168:171] offset:6144\n"
"s_cbranch_vccnz .Lg_slow_%=\n"
".Lg_slowret_%=:\n"
"s_waitcnt lgkmcnt(3)\n"
"v_cmp_gt_u32 vcc, s68, v245\n"
"v_pk_mul_f32 v[72:73], v[72:73], v[172:173] op_sel_hi:[1,0]\n"
"v_pk_mul_f32 v[74:75], v[74:75], v[172:173] op_sel_hi:[1,0]\n"
"v_pk_mul_f32 v[84:85], v[84:85], v[172:173] op_sel_hi:[1,0]\n"
"v_pk_mul_f32 v[86:87], v[86:87], v[172:173] op_sel_hi:[1,0]\n"
"v_cvt_pk_bf16_f32 v148, v72, v73\n"
"v_cvt_pk_bf16_f32 v149, v74, v75\n"
"v_cvt_pk_bf16_f32 v150, v84, v85\n"
"v_cvt_pk_bf16_f32 v151, v86, v87\n"
"s_cbranch_vccnz .Lg_spin9_%=\n"
".Lg_back10_%=:\n"
"ds_read_b64_tr_b16 v[212:213], v249 offset:0\n"
"ds_read_b64_tr_b16 v[214:215], v249 offset:256\n"
"ds_read_b64_tr_b16 v[216:217], v249 offset:512\n"
"ds_read_b64_tr_b16 v[218:219], v249 offset:768\n"
"ds_read_b64_tr_b16 v[220:221], v249 offset:8448\n"
"ds_read_b64_tr_b16 v[222:223], v249 offset:8704\n"
"ds_read_b64_tr_b16 v[224:225], v249 offset:8960\n"
"ds_read_b64_tr_b16 v[226:227], v249 offset:9216\n"
"ds_read_b64_tr_b16 v[228:229], v249 offset:16896\n"
"ds_read_b64_tr_b16 v[230:231], v249 offset:17152\n"
"ds_read_b64_tr_b16 v[232:233], v249 offset:17408\n"
"ds_read_b64_tr_b16 v[234:235], v249 offset:17664\n"
"ds_read_b64_tr_b16 v[236:237], v249 offset:25344\n"
"ds_read_b64_tr_b16 v[238:239], v249 offset:25600\n"
"ds_read_b64_tr_b16 v[240:241], v249 offset:25856\n"
"ds_read_b64_tr_b16 v[242:243], v249 offset:26112\n"
"ds_write_b128 v254, v[148:151] offset:9216\n"
"v_pk_mul_f32 v[4:5], v[4:5], v[174:175] op_sel_hi:[1,0]\n"
"v_pk_mul_f32 v[6:7], v[6:7], v[174:175] op_sel_hi:[1,0]\n"
"v_pk_mul_f32 v[16:17], v[16:17], v[174:175] op_sel_hi:[1,0]\n"
"v_pk_mul_f32 v[18:19], v[18:19], v[174:175] op_sel_hi:[1,0]\n"
"v_cvt_pk_bf16_f32 v116, v4, v5\n"
"v_cvt_pk_bf16_f32 v117, v6, v7\n"
"v_cvt_pk_bf16_f32 v118, v16, v17\n"
"v_cvt_pk_bf16_f32 v119, v18, v19\n"
"ds_write_b128 v254, v[116:119] offset:1024\n"
"v_pk_mul_f32 v[28:29], v[28:29], v[174:175] op_sel_hi:[1,0]\n"
"v_pk_mul_f32 v[30:31], v[30:31], v[174:175] op_sel_hi:[1,0]\n"
"v_pk_mul_f32 v[40:41], v[40:41], v[174:175] op_sel_hi:[1,0]\n"
"v_pk_mul_f32 v[42:43], v[42:43], v[174:175] op_sel_hi:[1,0]\n"
"v_cvt_pk_bf16_f32 v128, v28, v29\n"
"v_cvt_pk_bf16_f32 v129, v30, v31\n"
"v_cvt_pk_bf16_f32 v130, v40, v41\n"
"v_cvt_pk_bf16_f32 v131, v42, v43\n"
"ds_write_b128 v254, v[128:131] offset:4096\n"
"v_pk_mul_f32 v[52:53], v[52:53], v[174:175] op_sel_hi:[1,0]\n"
"v_pk_mul_f32 v[54:55], v[54:55], v[174:175] op_sel_hi:[1,0]\n"
"v_pk_mul_f32 v[64:65], v[64:65], v[174:175] op_sel_hi:[1,0]\n"
"v_pk_mul_f32 v[66:67], v[66:67], v[174:175] op_sel_hi:[1,0]\n"
"v_cvt_pk_bf16_f32 v140, v52, v53\n"
"v_cvt_pk_bf16_f32 v141, v54, v55\n"
"v_cvt_pk_bf16_f32 v142, v64, v65\n"
"v_cvt_pk_bf16_f32 v143, v66, v67\n"
"ds_write_b128 v254, v[140:143] offset:7168\n"
"v_pk_mul_f32 v[76:77], v[76:77], v[174:175] op_sel_hi:[1,0]\n"
"v_pk_mul_f32 v[78:79], v[78:79], v[174:175] op_sel_hi:[1,0]\n"
"v_pk_mul_f32 v[88:89], v[88:89], v[174:175] op_sel_hi:[1,0]\n"
"v_pk_mul_f32 v[90:91], v[90:91], v[174:175] op_sel_hi:[1,0]\n"
"v_cvt_pk_bf16_f32 v152, v76, v77\n"
"v_cvt_pk_bf16_f32 v153, v78, v79\n"
"v_cvt_pk_bf16_f32 v154, v88, v89\n"
"v_cvt_pk_bf16_f32 v155, v90, v91\n"
"ds_write_b128 v254, v[152:155] offset:10240\n"
"v_pk_mul_f32 v[8:9], v[8:9], v[176:177] op_sel_hi:[1,0]\n"
"v_pk_mul_f32 v[10:11], v[10:11], v[176:177] op_sel_hi:[1,0]\n"
"v_pk_mul_f32 v[20:21], v[20:21], v[176:177] op_sel_hi:[1,0]\n"
"v_pk_mul_f32 v[22:23], v[22:23], v[176:177] op_sel_hi:[1,0]\n"
"v_cvt_pk_bf16_f32 v120, v8, v9\n"
"v_cvt_pk_bf16_f32 v121, v10, v11\n"
"v_cvt_pk_bf16_f32 v122, v20, v21\n"
"v_cvt_pk_bf16_f32 v123, v22, v23\n"
"ds_write_b128 v254, v[120:123] offset:2048\n"
"v_pk_mul_f32 v[32:33], v[32:33], v[176:177] op_sel_hi:[1,0]\n"
"v_pk_mul_f32 v[34:35], v[34:35], v[176:177] op_sel_hi:[1,0]\n"
"v_pk_mul_f32 v[44:45], v[44:45], v[176:177] op_sel_hi:[1,0]\n"
"v_pk_mul_f32 v[46:47], v[46:47], v[176:177] op_sel_hi:[1,0]\n"
"v_cvt_pk_bf16_f32 v132, v32, v33\n"
"v_cvt_pk_bf16_f32 v133, v34, v35\n"
"v_cvt_pk_bf16_f32 v134, v44, v45\n"
"v_cvt_pk_bf16_f32 v135, v46, v47\n"
"ds_write_b128 v254, v[132:135] offset:5120\n"
"v_pk_mul_f32 v[56:57], v[56:57], v[176:177] op_sel_hi:[1,0]\n"
"v_pk_mul_f32 v[58:59], v[58:59], v[176:177] op_sel_hi:[1,0]\n"
"v_pk_mul_f32 v[68:69], v[68:69], v[176:177] op_sel_hi:[1,0]\n"
"v_pk_mul_f32 v[70:71], v[70:71], v[176:177] op_sel_hi:[1,0]\n"
"v_cvt_pk_bf16_f32 v144, v56, v57\n"
"v_cvt_pk_bf16_f32 v145, v58, v59\n"
"v_cvt_pk_bf16_f32 v146, v68, v69\n"
"v_cvt_pk_bf16_f32 v147, v70, v71\n"
"ds_write_b128 v254, v[144:147] offset:8192\n"
"v_pk_mul_f32 v[80:81], v[80:81], v[176:177] op_sel_hi:[1,0]\n"
"v_pk_mul_f32 v[82:83], v[82:83], v[176:177] op_sel_hi:[1,0]\n"
"v_pk_mul_f32 v[92:93], v[92:93], v[176:177] op_sel_hi:[1,0]\n"
"v_pk_mul_f32 v[94:95], v[94:95], v[176:177] op_sel_hi:[1,0]\n"
"v_cvt_pk_bf16_f32 v156, v80, v81\n"
"v_cvt_pk_bf16_f32 v157, v82, v83\n"
"v_cvt_pk_bf16_f32 v158, v92, v93\n"
"v_cvt_pk_bf16_f32 v159, v94, v95\n"
"ds_write_b128 v254, v[156:159] offset:11264\n"
"ds_add_u32 v246, v247\n"
"ds_read_b32 v245, v244 offset:1024\n"
"s_waitcnt lgkmcnt(0)\n"
"v_cmp_gt_u32 vcc, 17, v245\n"
"s_cbranch_vccnz .Lg_spin11_%=\n"
".Lg_back12_%=:\n"
"ds_read_b128 v[0:3], v251 offset:0\n"
"ds_read_b128 v[4:7], v251 offset:1024\n"
"ds_read_b128 v[24:27], v251 offset:3072\n"
"ds_read_b128 v[28:31], v251 offset:4096\n"
"ds_read_b128 v[48:51], v251 offset:6144\n"
"ds_read_b128 v[52:55], v251 offset:7168\n"
"ds_read_b128 v[72:75], v251 offset:9216\n"
"ds_read_b128 v[76:79], v251 offset:10240\n"
"s_waitcnt lgkmcnt(7)\n"
"v_mfma_f32_16x16x32_bf16 v[192:195], v[212:215], v[0:3], 0\n"
"ds_read_b128 v[96:99], v251 offset:24576\n"
"s_waitcnt lgkmcnt(7)\n"
"v_mfma_f32_16x16x32_bf16 v[196:199], v[212:215], v[4:7], 0\n"
"ds_read_b128 v[100:103], v251 offset:25600\n"
"s_waitcnt lgkmcnt(7)\n"
"v_mfma_f32_16x16x32_bf16 v[192:195], v[216:219], v[24:27], v[192:195]\n"
"ds_read_b128 v[120:123], v251 offset:27648\n"
"s_waitcnt lgkmcnt(7)\n"
"v_mfma_f32_16x16x32_bf16 v[196:199], v[216:219], v[28:31], v[196:199]\n"
"ds_read_b128 v[124:127], v251 offset:28672\n"
"s_waitcnt lgkmcnt(7)\n"
"v_mfma_f32_16x16x32_bf16 v[192:195], v[220:223], v[48:51], v[192:195]\n"
"ds_read_b128 v[144:147], v251 offset:30720\n"
"s_waitcnt lgkmcnt(7)\n"
"v_mfma_f32_16x16x32_bf16 v[196:199], v[220:223], v[52:55], v[196:199]\n"
"ds_read_b128 v[148:151], v251 offset:31744\n"
"s_waitcnt lgkmcnt(7)\n"
"v_mfma_f32_16x16x32_bf16 v[192:195], v[224:227], v[72:75], v[192:195]\n"
"ds_read_b128 v[168:171], v251 offset:33792\n"
"s_waitcnt lgkmcnt(7)\n"
"v_mfma_f32_16x16x32_bf16 v[196:199], v[224:227], v[76:79], v[196:199]\n"
"ds_read_b128 v[172:175], v251 offset:34816\n"
"s_waitcnt lgkmcnt(7)\n"
"v_mfma_f32_16x16x32_bf16 v[192:195], v[228:231], v[96:99], v[192:195]\n"
"ds_read_b128 v[8:11], v251 offset:2048\n"
"s_waitcnt lgkmcnt(7)\n"
"v_mfma_f32_16x16x32_bf16 v[196:199], v[228:231], v[100:103], v[196:199]\n"
"ds_read_b128 v[32:35], v251 offset:5120\n"
"s_waitcnt lgkmcnt(7)\n"
"v_mfma_f32_16x16x32_bf16 v[192:195], v[232:235], v[120:123], v[192:195]\n"
"ds_read_b128 v[56:59], v251 offset:8192\n"
"s_waitcnt lgkmcnt(7)\n"
"v_mfma_f32_16x16x32_bf16 v[196:199], v[232:235], v[124:127], v[196:199]\n"
"ds_read_b128 v[80:83], v251 offset:11264\n"
"s_waitcnt lgkmcnt(7)\n"
"v_mfma_f32_16x16x32_bf16 v[192:195], v[236:239], v[144:147], v[192:195]\n"
"ds_read_b128 v[104:107], v251 offset:26624\n"
"s_waitcnt lgkmcnt(7)\n"
"v_mfma_f32_16x16x32_bf16 v[196:199], v[236:239], v[148:151], v[196:199]\n"
"ds_read_b128 v[128:131], v251 offset:29696\n"
"s_waitcnt lgkmcnt(7)\n"
"v_mfma_f32_16x16x32_bf16 v[192:195], v[240:243], v[168:171], v[192:195]\n"
"ds_read_b128 v[152:155], v251 offset:32768\n"
"s_waitcnt lgkmcnt(7)\n"
"v_mfma_f32_16x16x32_bf16 v[196:199], v[240:243], v[172:175], v[196:199]\n"
"ds_read_b128 v[176:179], v251 offset:35840\n"
"s_waitcnt lgkmcnt(7)\n"
"s_add_u32 s68, s68, 1\n"
"v_mfma_f32_16x16x32_bf16 v[200:203], v[212:215], v[8:11], 0\n"
"ds_read_b128 v[12:15], v251 offset:12288\n"
"s_waitcnt lgkmcnt(7)\n"
"s_min_u32 s68, s68, 26\n"
"v_mfma_f32_16x16x32_bf16 v[200:203], v[216:219], v[32:35], v[200:203]\n"
"ds_read_b128 v[36:39], v251 offset:15360\n"
"s_waitcnt lgkmcnt(7)\n"
"v_mfma_f32_16x16x32_bf16 v[200:203], v[220:223], v[56:59], v[200:203]\n"
"ds_read_b128 v[60:63], v251 offset:18432\n"
"s_waitcnt lgkmcnt(7)\n"
"v_add_u32 v249, s70, v249\n"
"v_mfma_f32_16x16x32_bf16 v[200:203], v[224:227], v[80:83], v[200:203]\n"
"ds_read_b128 v[84:87], v251 offset:21504\n"
"s_waitcnt lgkmcnt(7)\n"
"ds_read_b32 v245, v244 offset:0\n"
"v_mfma_f32_16x16x32_bf16 v[200:203], v[228:231], v[104:107], v[200:203]\n"
"ds_read_b128 v[108:111], v251 offset:36864\n"
"s_waitcnt lgkmcnt(8)\n"
"v_cvt_pk_bf16_f32 v192, v192, v193\n"
"v_mfma_f32_16x16x32_bf16 v[200:203], v[232:235], v[128:131], v[200:203]\n"
"ds_read_b128 v[132:135], v251 offset:39936\n"
"s_waitcnt lgkmcnt(8)\n"
"v_cvt_pk_bf16_f32 v193, v194, v195\n"
"v_mfma_f32_16x16x32_bf16 v[200:203], v[236:239], v[152:155], v[200:203]\n"
"ds_read_b128 v[156:159], v251 offset:43008\n"
"s_waitcnt lgkmcnt(8)\n"
"v_cvt_pk_bf16_f32 v194, v196, v197\n"
"v_mfma_f32_16x16x32_bf16 v[200:203], v[240:243], v[176:179], v[200:203]\n"
"ds_read_b128 v[180:183], v251 offset:46080\n"
"s_waitcnt lgkmcnt(8)\n"
"v_cvt_pk_bf16_f32 v195, v198, v199\n"
"v_mfma_f32_16x16x32_bf16 v[196:199], v[212:215], v[12:15], 0\n"
"ds_read_b128 v[16:19], v251 offset:13312\n"
"s_waitcnt lgkmcnt(8)\n"
"v_mfma_f32_16x16x32_bf16 v[196:199], v[216:219], v[36:39], v[196:199]\n"
"ds_read_b128 v[20:23], v251 offset:14336\n"
"s_waitcnt lgkmcnt(8)\n"
"buffer_store_dwordx4 v[192:195], v248, s[48:51], s74 offen offset:0 sc1\n"
"v_mfma_f32_16x16x32_bf16 v[196:199], v[220:223], v[60:63], v[196:199]\n"
"ds_read_b128 v[40:43], v251 offset:16384\n"
"s_waitcnt lgkmcnt(8)\n"
"v_mfma_f32_16x16x32_bf16 v[196:199], v[224:227], v[84:87], v[196:199]\n"
"ds_read_b128 v[44:47], v251 offset:17408\n"
"s_waitcnt lgkmcnt(7)\n"
"v_cmp_gt_u32 vcc, s68, v245\n"
"v_mfma_f32_16x16x32_bf16 v[196:199], v[228:231], v[108:111], v[196:199]\n"
"ds_read_b128 v[64:67], v251 offset:19456\n"
"s_waitcnt lgkmcnt(7)\n"
"s_add_u32 s69, s69, 1\n"
"s_cmp_eq_u32 s69, 3\n"
"v_mfma_f32_16x16x32_bf16 v[196:199], v[232:235], v[132:135], v[196:199]\n"
"ds_read_b128 v[68:71], v251 offset:20480\n"
"s_waitcnt lgkmcnt(7)\n"
"s_cselect_b32 s70, -67584, s34\n"
"s_cselect_b32 s69, 0, s69\n"
"v_mfma_f32_16x16x32_bf16 v[196:199], v[236:239], v[156:159], v[196:199]\n"
"ds_read_b128 v[88:91], v251 offset:22528\n"
"s_waitcnt lgkmcnt(7)\n"
"v_mfma_f32_16x16x32_bf16 v[196:199], v[240:243], v[180:183], v[196:199]\n"
"ds_read_b128 v[92:95], v251 offset:23552\n"
"s_waitcnt lgkmcnt(7)\n"
"s_cbranch_vccnz .Lg_spin13_%=\n"
".Lg_back14_%=:\n"
"v_mfma_f32_16x16x32_bf16 v[204:207], v[212:215], v[16:19], 0\n"
"ds_read_b128 v[112:115], v251 offset:37888\n"
"s_waitcnt lgkmcnt(7)\n"
"v_mfma_f32_16x16x32_bf16 v[208:211], v[212:215], v[20:23], 0\n"
"ds_read_b128 v[116:119], v251 offset:38912\n"
"s_waitcnt lgkmcnt(7)\n"
"ds_read_b64_tr_b16 v[212:213], v249 offset:0\n"
"v_mfma_f32_16x16x32_bf16 v[204:207], v[216:219], v[40:43], v[204:207]\n"
"ds_read_b128 v[136:139], v251 offset:40960\n"
"s_waitcnt lgkmcnt(8)\n"
"ds_read_b64_tr_b16 v[214:215], v249 offset:256\n"
"v_cvt_pk_bf16_f32 v200, v200, v201\n"
"v_mfma_f32_16x16x32_bf16 v[208:211], v[216:219], v[44:47], v[208:211]\n"
"ds_read_b128 v[140:143], v251 offset:41984\n"
"s_waitcnt lgkmcnt(9)\n"
"ds_read_b64_tr_b16 v[216:217], v249 offset:512\n"
"v_cvt_pk_bf16_f32 v201, v202, v203\n"
"v_mfma_f32_16x16x32_bf16 v[204:207], v[220:223], v[64:67], v[204:207]\n"
"ds_read_b128 v[160:163], v251 offset:44032\n"
"s_waitcnt lgkmcnt(10)\n"
"ds_read_b64_tr_b16 v[218:219], v249 offset:768\n"
"v_cvt_pk_bf16_f32 v202, v196, v197\n"
"v_mfma_f32_16x16x32_bf16 v[208:211], v[220:223], v[68:71], v[208:211]\n"
"ds_read_b128 v[164:167], v251 offset:45056\n"
"s_waitcnt lgkmcnt(11)\n"
"ds_read_b64_tr_b16 v[220:221], v249 offset:8448\n"
"v_cvt_pk_bf16_f32 v203, v198, v199\n"
"v_mfma_f32_16x16x32_bf16 v[204:207], v[224:227], v[88:91], v[204:207]\n"
"ds_read_b128 v[184:187], v251 offset:47104\n"
"s_waitcnt lgkmcnt(12)\n"
"s_waitcnt vmcnt(7)\n"
"ds_read_b64_tr_b16 v[222:223], v249 offset:8704\n"
"ds_add_u32 v246, v247\n"
"v_mfma_f32_16x16x32_bf16 v[208:211], v[224:227], v[92:95], v[208:211]\n"
"ds_read_b128 v[188:191], v251 offset:48128\n"
"s_waitcnt lgkmcnt(14)\n"
"ds_read_b64_tr_b16 v[224:225], v249 offset:8960\n"
"buffer_store_dwordx4 v[200:203], v248, s[48:51], s74 offen offset:512 sc1\n"
"v_mfma_f32_16x16x32_bf16 v[204:207], v[228:231], v[112:115], v[204:207]\n"
"s_waitcnt lgkmcnt(14)\n"
"s_mov_b32 s71, s74\n"
"ds_read_b64_tr_b16 v[226:227], v249 offset:9216\n"
"v_mfma_f32_16x16x32_bf16 v[208:211], v[228:231], v[116:119], v[208:211]\n"
"s_waitcnt lgkmcnt(13)\n"
"s_add_u32 s74, s74, 196608\n"
"ds_read_b64_tr_b16 v[228:229], v249 offset:16896\n"
"v_mfma_f32_16x16x32_bf16 v[204:207], v[232:235], v[136:139], v[204:207]\n"
"s_waitcnt lgkmcnt(12)\n"
"s_cmp_eq_u32 s74, 1769472\n"
"ds_read_b64_tr_b16 v[230:231], v249 offset:17152\n"
"v_mfma_f32_16x16x32_bf16 v[208:211], v[232:235], v[140:143], v[208:211]\n"
"s_waitcnt lgkmcnt(11)\n"
"s_cselect_b32 s74, 0, s74\n"
"ds_read_b64_tr_b16 v[232:233], v249 offset:17408\n"
"v_mfma_f32_16x16x32_bf16 v[204:207], v[236:239], v[160:163], v[204:207]\n"
"s_waitcnt lgkmcnt(10)\n"
"ds_read_b64_tr_b16 v[234:235], v249 offset:17664\n"
"v_mfma_f32_16x16x32_bf16 v[208:211], v[236:239], v[164:167], v[208:211]\n"
"s_waitcnt lgkmcnt(9)\n"
"ds_read_b64_tr_b16 v[236:237], v249 offset:25344\n"
"v_mfma_f32_16x16x32_bf16 v[204:207], v[240:243], v[184:187], v[204:207]\n"
"s_waitcnt lgkmcnt(7)\n"
"ds_read_b64_tr_b16 v[238:239], v249 offset:25600\n"
"v_mfma_f32_16x16x32_bf16 v[208:211], v[240:243], v[188:191], v[208:211]\n"
"ds_read_b64_tr_b16 v[240:241], v249 offset:25856\n"
"ds_read_b64_tr_b16 v[242:243], v249 offset:26112\n"
".Lg_p3loop_%=:\n"
"s_waitcnt lgkmcnt(15)\n"
"v_mfma_f32_16x16x32_bf16 v[192:195], v[212:215], v[0:3], 0\n"
"v_mfma_f32_16x16x32_bf16 v[196:199], v[212:215], v[4:7], 0\n"
"s_waitcnt lgkmcnt(13)\n"
"v_mfma_f32_16x16x32_bf16 v[192:195], v[216:219], v[24:27], v[192:195]\n"
"v_mfma_f32_16x16x32_bf16 v[196:199], v[216:219], v[28:31], v[196:199]\n"
"s_waitcnt lgkmcnt(11)\n"
"v_cvt_pk_bf16_f32 v204, v204, v205\n"
"v_mfma_f32_16x16x32_bf16 v[192:195], v[220:223], v[48:51], v[192:195]\n"
"v_cvt_pk_bf16_f32 v205, v206, v207\n"
"v_mfma_f32_16x16x32_bf16 v[196:199], v[220:223], v[52:55], v[196:199]\n"
"s_waitcnt lgkmcnt(8)\n"
"v_cvt_pk_bf16_f32 v206, v208, v209\n"
"v_mfma_f32_16x16x32_bf16 v[192:195], v[224:227], v[72:75], v[192:195]\n"
"v_cvt_pk_bf16_f32 v207, v210, v211\n"
"v_mfma_f32_16x16x32_bf16 v[196:199], v[224:227], v[76:79], v[196:199]\n"
"s_waitcnt lgkmcnt(6)\n"
"v_mfma_f32_16x16x32_bf16 v[192:195], v[228:231], v[96:99], v[192:195]\n"
"buffer_store_dwordx4 v[204:207], v248, s[48:51], s71 offen offset:1024 sc1\n"
"v_mfma_f32_16x16x32_bf16 v[196:199], v[228:231], v[100:103], v[196:199]\n"
"s_waitcnt lgkmcnt(4)\n"
"v_mfma_f32_16x16x32_bf16 v[192:195], v[232:235], v[120:123], v[192:195]\n"
"v_mfma_f32_16x16x32_bf16 v[196:199], v[232:235], v[124:127], v[196:199]\n"
"s_waitcnt lgkmcnt(2)\n"
"v_mfma_f32_16x16x32_bf16 v[192:195], v[236:239], v[144:147], v[192:195]\n"
"v_mfma_f32_16x16x32_bf16 v[196:199], v[236:239], v[148:151], v[196:199]\n"
"s_waitcnt lgkmcnt(0)\n"
"v_mfma_f32_16x16x32_bf16 v[192:195], v[240:243], v[168:171], v[192:195]\n"
"v_mfma_f32_16x16x32_bf16 v[196:199], v[240:243], v[172:175], v[196:199]\n"
"s_add_u32 s68, s68, 1\n"
"v_mfma_f32_16x16x32_bf16 v[200:203], v[212:215], v[8:11], 0\n"
"s_min_u32 s68, s68, 26\n"
"v_mfma_f32_16x16x32_bf16 v[200:203], v[216:219], v[32:35], v[200:203]\n"
"s_add_u32 s73, s73, 1\n"
"v_mfma_f32_16x16x32_bf16 v[200:203], v[220:223], v[56:59], v[200:203]\n"
"v_add_u32 v249, s70, v249\n"
"v_mfma_f32_16x16x32_bf16 v[200:203], v[224:227], v[80:83], v[200:203]\n"
"ds_read_b32 v245, v244 offset:0\n"
"v_mfma_f32_16x16x32_bf16 v[200:203], v[228:231], v[104:107], v[200:203]\n"
"v_cvt_pk_bf16_f32 v192, v192, v193\n"
"v_mfma_f32_16x16x32_bf16 v[200:203], v[232:235], v[128:131], v[200:203]\n"
"v_cvt_pk_bf16_f32 v193, v194, v195\n"
"v_mfma_f32_16x16x32_bf16 v[200:203], v[236:239], v[152:155], v[200:203]\n"
"v_cvt_pk_bf16_f32 v194, v196, v197\n"
"v_mfma_f32_16x16x32_bf16 v[200:203], v[240:243], v[176:179], v[200:203]\n"
"v_cvt_pk_bf16_f32 v195, v198, v199\n"
"v_mfma_f32_16x16x32_bf16 v[196:199], v[212:215], v[12:15], 0\n"
"v_mfma_f32_16x16x32_bf16 v[196:199], v[216:219], v[36:39], v[196:199]\n"
"buffer_store_dwordx4 v[192:195], v248, s[48:51], s74 offen offset:0 sc1\n"
"v_mfma_f32_16x16x32_bf16 v[196:199], v[220:223], v[60:63], v[196:199]\n"
"v_mfma_f32_16x16x32_bf16 v[196:199], v[224:227], v[84:87], v[196:199]\n"
"s_waitcnt lgkmcnt(0)\n"
"v_cmp_gt_u32 vcc, s68, v245\n"
"v_mfma_f32_16x16x32_bf16 v[196:199], v[228:231], v[108:111], v[196:199]\n"
"s_add_u32 s69, s69, 1\n"
"s_cmp_eq_u32 s69, 3\n"
"v_mfma_f32_16x16x32_bf16 v[196:199], v[232:235], v[132:135], v[196:199]\n"
"s_cselect_b32 s70, -67584, s34\n"
"s_cselect_b32 s69, 0, s69\n"
"v_mfma_f32_16x16x32_bf16 v[196:199], v[236:239], v[156:159], v[196:199]\n"
"v_mfma_f32_16x16x32_bf16 v[196:199], v[240:243], v[180:183], v[196:199]\n"
"s_cbranch_vccnz .Lg_spin15_%=\n"
".Lg_back16_%=:\n"
"v_mfma_f32_16x16x32_bf16 v[204:207], v[212:215], v[16:19], 0\n"
"v_mfma_f32_16x16x32_bf16 v[208:211], v[212:215], v[20:23], 0\n"
"ds_read_b64_tr_b16 v[212:213], v249 offset:0\n"
"v_mfma_f32_16x16x32_bf16 v[204:207], v[216:219], v[40:43], v[204:207]\n"
"ds_read_b64_tr_b16 v[214:215], v249 offset:256\n"
"v_cvt_pk_bf16_f32 v200, v200, v201\n"
"v_mfma_f32_16x16x32_bf16 v[208:211], v[216:219], v[44:47], v[208:211]\n"
"ds_read_b64_tr_b16 v[216:217], v249 offset:512\n"
"v_cvt_pk_bf16_f32 v201, v202, v203\n"
"v_mfma_f32_16x16x32_bf16 v[204:207], v[220:223], v[64:67], v[204:207]\n"
"ds_read_b64_tr_b16 v[218:219], v249 offset:768\n"
"v_cvt_pk_bf16_f32 v202, v196, v197\n"
"v_mfma_f32_16x16x32_bf16 v[208:211], v[220:223], v[68:71], v[208:211]\n"
"ds_read_b64_tr_b16 v[220:221], v249 offset:8448\n"
"v_cvt_pk_bf16_f32 v203, v198, v199\n"
"v_mfma_f32_16x16x32_bf16 v[204:207], v[224:227], v[88:91], v[204:207]\n"
"s_waitcnt vmcnt(7)\n"
"ds_read_b64_tr_b16 v[222:223], v249 offset:8704\n"
"ds_add_u32 v246, v247\n"
"v_mfma_f32_16x16x32_bf16 v[208:211], v[224:227], v[92:95], v[208:211]\n"
"ds_read_b64_tr_b16 v[224:225], v249 offset:8960\n"
"buffer_store_dwordx4 v[200:203], v248, s[48:51], s74 offen offset:512 sc1\n"
"v_mfma_f32_16x16x32_bf16 v[204:207], v[228:231], v[112:115], v[204:207]\n"
"s_mov_b32 s71, s74\n"
"ds_read_b64_tr_b16 v[226:227], v249 offset:9216\n"
"v_mfma_f32_16x16x32_bf16 v[208:211], v[228:231], v[116:119], v[208:211]\n"
"s_add_u32 s74, s74, 196608\n"
"ds_read_b64_tr_b16 v[228:229], v249 offset:16896\n"
"v_mfma_f32_16x16x32_bf16 v[204:207], v[232:235], v[136:139], v[204:207]\n"
"s_cmp_eq_u32 s74, 1769472\n"
"ds_read_b64_tr_b16 v[230:231], v249 offset:17152\n"
"v_mfma_f32_16x16x32_bf16 v[208:211], v[232:235], v[140:143], v[208:211]\n"
"s_cselect_b32 s74, 0, s74\n"
"ds_read_b64_tr_b16 v[232:233], v249 offset:17408\n"
"v_mfma_f32_16x16x32_bf16 v[204:207], v[236:239], v[160:163], v[204:207]\n"
"ds_read_b64_tr_b16 v[234:235], v249 offset:17664\n"
"v_mfma_f32_16x16x32_bf16 v[208:211], v[236:239], v[164:167], v[208:211]\n"
"ds_read_b64_tr_b16 v[236:237], v249 offset:25344\n"
"v_mfma_f32_16x16x32_bf16 v[204:207], v[240:243], v[184:187], v[204:207]\n"
"s_cmp_lt_u32 s73, 9\n"
"ds_read_b64_tr_b16 v[238:239], v249 offset:25600\n"
"v_mfma_f32_16x16x32_bf16 v[208:211], v[240:243], v[188:191], v[208:211]\n"
"ds_read_b64_tr_b16 v[240:241], v249 offset:25856\n"
"ds_read_b64_tr_b16 v[242:243], v249 offset:26112\n"
"s_cbranch_scc1 .Lg_p3loop_%=\n"
"s_nop 5\n"
"v_cvt_pk_bf16_f32 v204, v204, v205\n"
"v_cvt_pk_bf16_f32 v205, v206, v207\n"
"v_cvt_pk_bf16_f32 v206, v208, v209\n"
"v_cvt_pk_bf16_f32 v207, v210, v211\n"
"buffer_store_dwordx4 v[204:207], v248, s[48:51], s71 offen offset:1024 sc1\n"
"ds_add_u32 v246, v247\n"
"s_waitcnt vmcnt(0)\n"
"ds_add_u32 v246, v247\n"
"v_readfirstlane_b32 s80, v252\n"
"v_readfirstlane_b32 s81, v253\n"
"s_mul_i32 s65, s80, 0x9e3779b1\n"
"s_add_u32 s65, s65, s81\n"
"s_add_u32 s65, s65, 0x7f4a7c15\n"
"s_lshr_b32 s66, s65, 15\n"
"s_xor_b32 s65, s65, s66\n"
"s_mul_i32 s65, s65, 0x85ebca77\n"
"s_lshr_b32 s66, s65, 13\n"
"s_xor_b32 s65, s65, s66\n"
"s_xor_b32 s66, s81, s65\n"
"s_mul_i32 s66, s66, 0xc2b2ae3d\n"
"s_add_u32 s66, s66, s80\n"
"s_mov_b32 s81, s65\n"
"s_lshr_b32 s65, s66, 16\n"
"s_xor_b32 s66, s66, s65\n"
"s_mul_i32 s66, s66, 0x165667b1\n"
"s_lshr_b32 s65, s66, 15\n"
"s_xor_b32 s66, s66, s65\n"
"s_and_b32 s80, s66, 0xfffffff0\n"
"s_cmp_lg_u32 s60, 0\n"
"s_cbranch_scc1 .Lg_npl17_%=\n"
"s_sleep 32\n"
"s_add_u32 s78, s80, 2\n"
"s_mov_b32 s79, s81\n"
"s_mov_b32 s65, 1024\n"
"v_mbcnt_lo_u32_b32 v2, -1, 0\n"
"v_mbcnt_hi_u32_b32 v2, -1, v2\n"
"v_and_b32 v2, 15, v2\n"
"v_lshlrev_b32 v2, 3, v2\n"
".Lg_pvec20_%=:\n"
"buffer_load_dwordx2 v[0:1], v2, s[36:39], 0 offen offset:1792 sc1\n"
"s_sub_u32 s61, s61, 16\n"
"s_cbranch_scc1 .Lg_exit_%=\n"
"s_waitcnt vmcnt(0)\n"
"v_cmp_ne_u64 vcc, s[78:79], v[0:1]\n"
"s_cbranch_vccnz .Lg_pvec20_%=\n"
".Lg_pok19_%=:\n"
"v_mov_b32 v4, 160768\n"
"v_mov_b32 v5, 65\n"
"ds_max_u32 v4, v5\n"
"v_mov_b32 v0, 0\n"
"v_mov_b32 v1, 0\n"
"buffer_store_dwordx2 v[0:1], v2, s[36:39], 0 offen offset:1792 sc1\n"
"s_waitcnt lgkmcnt(0)\n"
".Lg_npl17_%=:\n"
".Lg_cdone_%=:\n"
".Lg_exit_%=:\n"
"s_endpgm\n"
".Lg_spin1_%=:\n"
"s_sub_u32 s61, s61, 1\n"
"s_cbranch_scc1 .Lg_exit_%=\n"
"ds_read_b32 v245, v244 offset:0\n"
"s_waitcnt lgkmcnt(0)\n"
"v_cmp_gt_u32 vcc, s68, v245\n"
"s_cbranch_vccnz .Lg_spin1_%=\n"
"s_branch .Lg_back2_%=\n"
".Lg_spin3_%=:\n"
"s_sub_u32 s61, s61, 1\n"
"s_cbranch_scc1 .Lg_exit_%=\n"
"ds_read_b32 v245, v244 offset:0\n"
"s_waitcnt lgkmcnt(0)\n"
"v_cmp_gt_u32 vcc, s68, v245\n"
"s_cbranch_vccnz .Lg_spin3_%=\n"
"s_branch .Lg_back4_%=\n"
".Lg_spin5_%=:\n"
"s_sub_u32 s61, s61, 1\n"
"s_cbranch_scc1 .Lg_exit_%=\n"
"ds_read_b32 v245, v244 offset:0\n"
"s_waitcnt lgkmcnt(0)\n"
"v_cmp_gt_u32 vcc, s68, v245\n"
"s_cbranch_vccnz .Lg_spin5_%=\n"
"s_branch .Lg_back6_%=\n"
".Lg_spin7_%=:\n"
"s_sub_u32 s61, s61, 1\n"
"s_cbranch_scc1 .Lg_exit_%=\n"
"ds_read_b32 v245, v244 offset:1024\n"
"s_waitcnt lgkmcnt(0)\n"
"v_cmp_gt_u32 vcc, 9, v245\n"
"s_cbranch_vccnz .Lg_spin7_%=\n"
"s_branch .Lg_back8_%=\n"
".Lg_spin9_%=:\n"
"s_sub_u32 s61, s61, 1\n"
"s_cbranch_scc1 .Lg_exit_%=\n"
"ds_read_b32 v245, v244 offset:0\n"
"s_waitcnt lgkmcnt(0)\n"
"v_cmp_gt_u32 vcc, s68, v245\n"
"s_cbranch_vccnz .Lg_spin9_%=\n"
"s_branch .Lg_back10_%=\n"
".Lg_spin11_%=:\n"
"s_sub_u32 s61, s61, 1\n"
"s_cbranch_scc1 .Lg_exit_%=\n"
"ds_read_b32 v245, v244 offset:1024\n"
"s_waitcnt lgkmcnt(0)\n"
"v_cmp_gt_u32 vcc, 17, v245\n"
"s_cbranch_vccnz .Lg_spin11_%=\n"
"s_branch .Lg_back12_%=\n"
".Lg_spin13_%=:\n"
"s_sub_u32 s61, s61, 1\n"
"s_cbranch_scc1 .Lg_exit_%=\n"
"ds_read_b32 v245, v244 offset:0\n"
"s_waitcnt lgkmcnt(0)\n"
"v_cmp_gt_u32 vcc, s68, v245\n"
"s_cbranch_vccnz .Lg_spin13_%=\n"
"s_branch .Lg_back14_%=\n"
".Lg_spin15_%=:\n"
"s_sub_u32 s61, s61, 1\n"
"s_cbranch_scc1 .Lg_exit_%=\n"
"ds_read_b32 v245, v244 offset:0\n"
"s_waitcnt lgkmcnt(0)\n"
"v_cmp_gt_u32 vcc, s68, v245\n"
"s_cbranch_vccnz .Lg_spin15_%=\n"
"s_branch .Lg_back16_%=\n"
".Lg_park21_%=:\n"
"s_waitcnt vmcnt(4)\n"
"v_mov_b32 v6, 9\n"
"ds_max_u32 v4, v6\n"
"ds_read_b32 v7, v5\n"
"s_waitcnt lgkmcnt(0)\n"
"v_cmp_gt_i32 vcc, 1, v7\n"
"s_cbranch_vccz .Lg_go22_%=\n"
"s_waitcnt vmcnt(0)\n"
"v_mov_b32 v6, 10\n"
"ds_max_u32 v4, v6\n"
".Lg_poll23_%=:\n"
"s_sub_u32 s61, s61, 1\n"
"s_cbranch_scc1 .Lg_exit_%=\n"
"s_sleep 1\n"
"ds_read_b32 v7, v5\n"
"s_waitcnt lgkmcnt(0)\n"
"v_cmp_gt_i32 vcc, 1, v7\n"
"s_cbranch_vccnz .Lg_poll23_%=\n"
"s_branch .Lg_go22_%=\n"
".Lg_park24_%=:\n"
"s_waitcnt vmcnt(4)\n"
"v_mov_b32 v6, 10\n"
"ds_max_u32 v4, v6\n"
"ds_read_b32 v7, v5\n"
"s_waitcnt lgkmcnt(0)\n"
"v_cmp_gt_i32 vcc, 2, v7\n"
"s_cbranch_vccz .Lg_go25_%=\n"
"s_waitcnt vmcnt(0)\n"
"v_mov_b32 v6, 11\n"
"ds_max_u32 v4, v6\n"
".Lg_poll26_%=:\n"
"s_sub_u32 s61, s61, 1\n"
"s_cbranch_scc1 .Lg_exit_%=\n"
"s_sleep 1\n"
"ds_read_b32 v7, v5\n"
"s_waitcnt lgkmcnt(0)\n"
"v_cmp_gt_i32 vcc, 2, v7\n"
"s_cbranch_vccnz .Lg_poll26_%=\n"
"s_branch .Lg_go25_%=\n"
".Lg_park27_%=:\n"
"s_waitcnt vmcnt(4)\n"
"v_mov_b32 v6, 11\n"
"ds_max_u32 v4, v6\n"
"ds_read_b32 v7, v5\n"
"s_waitcnt lgkmcnt(0)\n"
"v_cmp_gt_i32 vcc, 3, v7\n"
"s_cbranch_vccz .Lg_go28_%=\n"
"s_waitcnt vmcnt(0)\n"
"v_mov_b32 v6, 12\n"
"ds_max_u32 v4, v6\n"
".Lg_poll29_%=:\n"
"s_sub_u32 s61, s61, 1\n"
"s_cbranch_scc1 .Lg_exit_%=\n"
"s_sleep 1\n"
"ds_read_b32 v7, v5\n"
"s_waitcnt lgkmcnt(0)\n"
"v_cmp_gt_i32 vcc, 3, v7\n"
"s_cbranch_vccnz .Lg_poll29_%=\n"
"s_branch .Lg_go28_%=\n"
".Lg_park30_%=:\n"
"s_waitcnt vmcnt(4)\n"
"v_mov_b32 v6, 12\n"
"ds_max_u32 v4, v6\n"
"ds_read_b32 v7, v5\n"
"s_waitcnt lgkmcnt(0)\n"
"v_cmp_gt_i32 vcc, 4, v7\n"
"s_cbranch_vccz .Lg_go31_%=\n"
"s_waitcnt vmcnt(0)\n"
"v_mov_b32 v6, 13\n"
"ds_max_u32 v4, v6\n"
".Lg_poll32_%=:\n"
"s_sub_u32 s61, s61, 1\n"
"s_cbranch_scc1 .Lg_exit_%=\n"
"s_sleep 1\n"
"ds_read_b32 v7, v5\n"
"s_waitcnt lgkmcnt(0)\n"
"v_cmp_gt_i32 vcc, 4, v7\n"
"s_cbranch_vccnz .Lg_poll32_%=\n"
"s_branch .Lg_go31_%=\n"
".Lg_park33_%=:\n"
"s_waitcnt vmcnt(4)\n"
"v_mov_b32 v6, 13\n"
"ds_max_u32 v4, v6\n"
"ds_read_b32 v7, v5\n"
"s_waitcnt lgkmcnt(0)\n"
"v_cmp_gt_i32 vcc, 5, v7\n"
"s_cbranch_vccz .Lg_go34_%=\n"
"s_waitcnt vmcnt(0)\n"
"v_mov_b32 v6, 14\n"
"ds_max_u32 v4, v6\n"
".Lg_poll35_%=:\n"
"s_sub_u32 s61, s61, 1\n"
"s_cbranch_scc1 .Lg_exit_%=\n"
"s_sleep 1\n"
"ds_read_b32 v7, v5\n"
"s_waitcnt lgkmcnt(0)\n"
"v_cmp_gt_i32 vcc, 5, v7\n"
"s_cbranch_vccnz .Lg_poll35_%=\n"
"s_branch .Lg_go34_%=\n"
".Lg_park36_%=:\n"
"s_waitcnt vmcnt(4)\n"
"v_mov_b32 v6, 14\n"
"ds_max_u32 v4, v6\n"
"ds_read_b32 v7, v5\n"
"s_waitcnt lgkmcnt(0)\n"
"v_cmp_gt_i32 vcc, 6, v7\n"
"s_cbranch_vccz .Lg_go37_%=\n"
"s_waitcnt vmcnt(0)\n"
"v_mov_b32 v6, 15\n"
"ds_max_u32 v4, v6\n"
".Lg_poll38_%=:\n"
"s_sub_u32 s61, s61, 1\n"
"s_cbranch_scc1 .Lg_exit_%=\n"
"s_sleep 1\n"
"ds_read_b32 v7, v5\n"
"s_waitcnt lgkmcnt(0)\n"
"v_cmp_gt_i32 vcc, 6, v7\n"
"s_cbranch_vccnz .Lg_poll38_%=\n"
"s_branch .Lg_go37_%=\n"
".Lg_park39_%=:\n"
"s_waitcnt vmcnt(4)\n"
"v_mov_b32 v6, 15\n"
"ds_max_u32 v4, v6\n"
"ds_read_b32 v7, v5\n"
"s_waitcnt lgkmcnt(0)\n"
"v_cmp_gt_i32 vcc, 7, v7\n"
"s_cbranch_vccz .Lg_go40_%=\n"
"s_waitcnt vmcnt(0)\n"
"v_mov_b32 v6, 16\n"
"ds_max_u32 v4, v6\n"
".Lg_poll41_%=:\n"
"s_sub_u32 s61, s61, 1\n"
"s_cbranch_scc1 .Lg_exit_%=\n"
"s_sleep 1\n"
"ds_read_b32 v7, v5\n"
"s_waitcnt lgkmcnt(0)\n"
"v_cmp_gt_i32 vcc, 7, v7\n"
"s_cbranch_vccnz .Lg_poll41_%=\n"
"s_branch .Lg_go40_%=\n"
".Lg_park42_%=:\n"
"s_waitcnt vmcnt(6)\n"
"v_mov_b32 v6, 15\n"
"ds_max_u32 v4, v6\n"
"ds_read_b32 v7, v5\n"
"s_waitcnt lgkmcnt(0)\n"
"v_cmp_gt_i32 vcc, 11, v7\n"
"s_cbranch_vccz .Lg_go43_%=\n"
"s_waitcnt vmcnt(2)\n"
"v_mov_b32 v6, 16\n"
"ds_max_u32 v4, v6\n"
"ds_read_b32 v7, v5\n"
"s_waitcnt lgkmcnt(0)\n"
"v_cmp_gt_i32 vcc, 11, v7\n"
"s_cbranch_vccz .Lg_go43_%=\n"
"s_waitcnt vmcnt(0)\n"
"v_mov_b32 v6, 17\n"
"ds_max_u32 v4, v6\n"
".Lg_poll44_%=:\n"
"s_sub_u32 s61, s61, 1\n"
"s_cbranch_scc1 .Lg_exit_%=\n"
"s_sleep 1\n"
"ds_read_b32 v7, v5\n"
"s_waitcnt lgkmcnt(0)\n"
"v_cmp_gt_i32 vcc, 11, v7\n"
"s_cbranch_vccnz .Lg_poll44_%=\n"
"s_branch .Lg_go43_%=\n"
".Lg_park45_%=:\n"
".Lg_poll47_%=:\n"
"s_sub_u32 s61, s61, 1\n"
"s_cbranch_scc1 .Lg_exit_%=\n"
"s_sleep 1\n"
"ds_read_b32 v7, v5\n"
"s_waitcnt lgkmcnt(0)\n"
"v_cmp_gt_i32 vcc, 13, v7\n"
"s_cbranch_vccnz .Lg_poll47_%=\n"
"s_branch .Lg_go46_%=\n"
".Lg_park48_%=:\n"
".Lg_poll50_%=:\n"
"s_sub_u32 s61, s61, 1\n"
"s_cbranch_scc1 .Lg_exit_%=\n"
"s_sleep 1\n"
"ds_read_b32 v7, v5\n"
"s_waitcnt lgkmcnt(0)\n"
"v_cmp_gt_i32 vcc, 15, v7\n"
"s_cbranch_vccnz .Lg_poll50_%=\n"
"s_branch .Lg_go49_%=\n"
".Lg_park51_%=:\n"
".Lg_poll53_%=:\n"
"s_sub_u32 s61, s61, 1\n"
"s_cbranch_scc1 .Lg_exit_%=\n"
"s_sleep 1\n"
"ds_read_b32 v7, v5\n"
"s_waitcnt lgkmcnt(0)\n"
"v_cmp_gt_i32 vcc, 18, v7\n"
"s_cbranch_vccnz .Lg_poll53_%=\n"
"s_branch .Lg_go52_%=\n"
".Lg_park54_%=:\n"
".Lg_poll56_%=:\n"
"s_sub_u32 s61, s61, 1\n"
"s_cbranch_scc1 .Lg_exit_%=\n"
"s_sleep 1\n"
"ds_read_b32 v7, v5\n"
"s_waitcnt lgkmcnt(0)\n"
"v_cmp_gt_i32 vcc, 19, v7\n"
"s_cbranch_vccnz .Lg_poll56_%=\n"
"s_branch .Lg_go55_%=\n"
".Lg_park57_%=:\n"
".Lg_poll59_%=:\n"
"s_sub_u32 s61, s61, 1\n"
"s_cbranch_scc1 .Lg_exit_%=\n"
"s_sleep 1\n"
"ds_read_b32 v7, v5\n"
"s_waitcnt lgkmcnt(0)\n"
"v_cmp_gt_i32 vcc, 20, v7\n"
"s_cbranch_vccnz .Lg_poll59_%=\n"
"s_branch .Lg_go58_%=\n"
".Lg_park60_%=:\n"
".Lg_poll62_%=:\n"
"s_sub_u32 s61, s61, 1\n"
"s_cbranch_scc1 .Lg_exit_%=\n"
"s_sleep 1\n"
"ds_read_b32 v7, v5\n"
"s_waitcnt lgkmcnt(0)\n"
"v_cmp_gt_i32 vcc, 21, v7\n"
"s_cbranch_vccnz .Lg_poll62_%=\n"
"s_branch .Lg_go61_%=\n"
".Lg_park63_%=:\n"
".Lg_poll65_%=:\n"
"s_sub_u32 s61, s61, 1\n"
"s_cbranch_scc1 .Lg_exit_%=\n"
"s_sleep 1\n"
"ds_read_b32 v7, v5\n"
"s_waitcnt lgkmcnt(0)\n"
"v_cmp_gt_i32 vcc, 22, v7\n"
"s_cbranch_vccnz .Lg_poll65_%=\n"
"s_branch .Lg_go64_%=\n"
".Lg_park66_%=:\n"
".Lg_poll68_%=:\n"
"s_sub_u32 s61, s61, 1\n"
"s_cbranch_scc1 .Lg_exit_%=\n"
"s_sleep 1\n"
"ds_read_b32 v7, v5\n"
"s_waitcnt lgkmcnt(0)\n"
"v_cmp_gt_i32 vcc, 23, v7\n"
"s_cbranch_vccnz .Lg_poll68_%=\n"
"s_branch .Lg_go67_%=\n"
".Lg_end_%=:\n"
".Lg_slow_%=:\n"
"s_nop 9\n"
"v_mbcnt_lo_u32_b32 v242, -1, 0\n"
"v_mbcnt_hi_u32_b32 v242, -1, v242\n"
"v_and_b32 v243, 15, v242\n"
"v_lshrrev_b32 v242, 4, v242\n"
"v_lshlrev_b32 v240, 8, v243\n"
"v_lshl_add_u32 v240, v242, 4, v240\n"
"v_lshlrev_b32 v241, 10, v242\n"
"v_lshl_add_u32 v241, v243, 1, v241\n"
"v_mov_b32 v184, 0xff800000\n"
"v_mov_b32 v188, 0xff800000\n"
"v_mov_b32 v192, 0xff800000\n"
"s_mov_b32 s63, 0\n"
".Lg_slmax_%=:\n"
"buffer_load_dwordx4 v[208:211], v240, s[40:43], s63 offen offset:0\n"
"buffer_load_dwordx4 v[212:215], v240, s[40:43], s63 offen offset:64\n"
"buffer_load_dwordx4 v[216:219], v240, s[40:43], s63 offen offset:128\n"
"buffer_load_dwordx4 v[220:223], v240, s[40:43], s63 offen offset:192\n"
"s_waitcnt vmcnt(0)\n"
"v_mfma_f32_16x16x32_bf16 v[172:175], v[208:211], v[112:115], 0\n"
"v_mfma_f32_16x16x32_bf16 v[172:175], v[212:215], v[124:127], v[172:175]\n"
"v_mfma_f32_16x16x32_bf16 v[172:175], v[216:219], v[136:139], v[172:175]\n"
"v_mfma_f32_16x16x32_bf16 v[172:175], v[220:223], v[148:151], v[172:175]\n"
"v_mfma_f32_16x16x32_bf16 v[176:179], v[208:211], v[116:119], 0\n"
"v_mfma_f32_16x16x32_bf16 v[176:179], v[212:215], v[128:131], v[176:179]\n"
"v_mfma_f32_16x16x32_bf16 v[176:179], v[216:219], v[140:143], v[176:179]\n"
"v_mfma_f32_16x16x32_bf16 v[176:179], v[220:223], v[152:155], v[176:179]\n"
"v_mfma_f32_16x16x32_bf16 v[180:183], v[208:211], v[120:123], 0\n"
"v_mfma_f32_16x16x32_bf16 v[180:183], v[212:215], v[132:135], v[180:183]\n"
"v_mfma_f32_16x16x32_bf16 v[180:183], v[216:219], v[144:147], v[180:183]\n"
"v_mfma_f32_16x16x32_bf16 v[180:183], v[220:223], v[156:159], v[180:183]\n"
"s_nop 9\n"
"v_max3_f32 v184, v184, v172, v173\n"
"v_max3_f32 v184, v184, v174, v175\n"
"v_max3_f32 v188, v188, v176, v177\n"
"v_max3_f32 v188, v188, v178, v179\n"
"v_max3_f32 v192, v192, v180, v181\n"
"v_max3_f32 v192, v192, v182, v183\n"
"s_add_u32 s63, s63, 4096\n"
"s_cmp_lt_u32 s63, 131072\n"
"s_cbranch_scc1 .Lg_slmax_%=\n"
"v_mov_b32 v185, v184\n"
"v_mov_b32 v189, v188\n"
"v_mov_b32 v193, v192\n"
"v_permlane32_swap_b32 v184, v185\n"
"v_permlane32_swap_b32 v188, v189\n"
"v_permlane32_swap_b32 v192, v193\n"
"v_max_f32 v184, v184, v185\n"
"v_max_f32 v188, v188, v189\n"
"v_max_f32 v192, v192, v193\n"
"v_mov_b32 v185, v184\n"
"v_mov_b32 v189, v188\n"
"v_mov_b32 v193, v192\n"
"v_permlane16_swap_b32 v184, v185\n"
"v_permlane16_swap_b32 v188, v189\n"
"v_permlane16_swap_b32 v192, v193\n"
"v_max_f32 v184, v184, v185\n"
"v_max_f32 v188, v188, v189\n"
"v_max_f32 v192, v192, v193\n"
"v_sub_f32 v160, 0, v184\n"
"v_sub_f32 v161, 0, v184\n"
"v_sub_f32 v162, 0, v184\n"
"v_sub_f32 v163, 0, v184\n"
"v_sub_f32 v164, 0, v188\n"
"v_sub_f32 v165, 0, v188\n"
"v_sub_f32 v166, 0, v188\n"
"v_sub_f32 v167, 0, v188\n"
"v_sub_f32 v168, 0, v192\n"
"v_sub_f32 v169, 0, v192\n"
"v_sub_f32 v170, 0, v192\n"
"v_sub_f32 v171, 0, v192\n"
"v_mov_b32 v0, 0\n"
"v_mov_b32 v1, 0\n"
"v_mov_b32 v2, 0\n"
"v_mov_b32 v3, 0\n"
"v_mov_b32 v4, 0\n"
"v_mov_b32 v5, 0\n"
"v_mov_b32 v6, 0\n"
"v_mov_b32 v7, 0\n"
"v_mov_b32 v8, 0\n"
"v_mov_b32 v9, 0\n"
"v_mov_b32 v10, 0\n"
"v_mov_b32 v11, 0\n"
"v_mov_b32 v12, 0\n"
"v_mov_b32 v13, 0\n"
"v_mov_b32 v14, 0\n"
"v_mov_b32 v15, 0\n"
"v_mov_b32 v16, 0\n"
"v_mov_b32 v17, 0\n"
"v_mov_b32 v18, 0\n"
"v_mov_b32 v19, 0\n"
"v_mov_b32 v20, 0\n"
"v_mov_b32 v21, 0\n"
"v_mov_b32 v22, 0\n"
"v_mov_b32 v23, 0\n"
"v_mov_b32 v24, 0\n"
"v_mov_b32 v25, 0\n"
"v_mov_b32 v26, 0\n"
"v_mov_b32 v27, 0\n"
"v_mov_b32 v28, 0\n"
"v_mov_b32 v29, 0\n"
"v_mov_b32 v30, 0\n"
"v_mov_b32 v31, 0\n"
"v_mov_b32 v32, 0\n"
"v_mov_b32 v33, 0\n"
"v_mov_b32 v34, 0\n"
"v_mov_b32 v35, 0\n"
"v_mov_b32 v36, 0\n"
"v_mov_b32 v37, 0\n"
"v_mov_b32 v38, 0\n"
"v_mov_b32 v39, 0\n"
"v_mov_b32 v40, 0\n"
"v_mov_b32 v41, 0\n"
"v_mov_b32 v42, 0\n"
"v_mov_b32 v43, 0\n"
"v_mov_b32 v44, 0\n"
"v_mov_b32 v45, 0\n"
"v_mov_b32 v46, 0\n"
"v_mov_b32 v47, 0\n"
"v_mov_b32 v48, 0\n"
"v_mov_b32 v49, 0\n"
"v_mov_b32 v50, 0\n"
"v_mov_b32 v51, 0\n"
"v_mov_b32 v52, 0\n"
"v_mov_b32 v53, 0\n"
"v_mov_b32 v54, 0\n"
"v_mov_b32 v55, 0\n"
"v_mov_b32 v56, 0\n"
"v_mov_b32 v57, 0\n"
"v_mov_b32 v58, 0\n"
"v_mov_b32 v59, 0\n"
"v_mov_b32 v60, 0\n"
"v_mov_b32 v61, 0\n"
"v_mov_b32 v62, 0\n"
"v_mov_b32 v63, 0\n"
"v_mov_b32 v64, 0\n"
"v_mov_b32 v65, 0\n"
"v_mov_b32 v66, 0\n"
"v_mov_b32 v67, 0\n"
"v_mov_b32 v68, 0\n"
"v_mov_b32 v69, 0\n"
"v_mov_b32 v70, 0\n"
"v_mov_b32 v71, 0\n"
"v_mov_b32 v72, 0\n"
"v_mov_b32 v73, 0\n"
"v_mov_b32 v74, 0\n"
"v_mov_b32 v75, 0\n"
"v_mov_b32 v76, 0\n"
"v_mov_b32 v77, 0\n"
"v_mov_b32 v78, 0\n"
"v_mov_b32 v79, 0\n"
"v_mov_b32 v80, 0\n"
"v_mov_b32 v81, 0\n"
"v_mov_b32 v82, 0\n"
"v_mov_b32 v83, 0\n"
"v_mov_b32 v84, 0\n"
"v_mov_b32 v85, 0\n"
"v_mov_b32 v86, 0\n"
"v_mov_b32 v87, 0\n"
"v_mov_b32 v88, 0\n"
"v_mov_b32 v89, 0\n"
"v_mov_b32 v90, 0\n"
"v_mov_b32 v91, 0\n"
"v_mov_b32 v92, 0\n"
"v_mov_b32 v93, 0\n"
"v_mov_b32 v94, 0\n"
"v_mov_b32 v95, 0\n"
"v_mov_b32 v96, 0\n"
"v_mov_b32 v97, 0\n"
"v_mov_b32 v98, 0\n"
"v_mov_b32 v99, 0\n"
"v_mov_b32 v100, 0\n"
"v_mov_b32 v101, 0\n"
"v_mov_b32 v102, 0\n"
"v_mov_b32 v103, 0\n"
"v_mov_b32 v104, 0\n"
"v_mov_b32 v105, 0\n"
"v_mov_b32 v106, 0\n"
"v_mov_b32 v107, 0\n"
"s_mov_b32 s63, 0\n"
".Lg_slatt_%=:\n"
"s_add_u32 s64, s63, 4096\n"
"buffer_load_dwordx4 v[208:211], v240, s[40:43], s63 offen offset:0\n"
"buffer_load_dwordx4 v[212:215], v240, s[40:43], s63 offen offset:64\n"
"buffer_load_dwordx4 v[216:219], v240, s[40:43], s63 offen offset:128\n"
"buffer_load_dwordx4 v[220:223], v240, s[40:43], s63 offen offset:192\n"
"buffer_load_dwordx4 v[224:227], v240, s[40:43], s64 offen offset:0\n"
"buffer_load_dwordx4 v[228:231], v240, s[40:43], s64 offen offset:64\n"
"buffer_load_dwordx4 v[232:235], v240, s[40:43], s64 offen offset:128\n"
"buffer_load_dwordx4 v[236:239], v240, s[40:43], s64 offen offset:192\n"
"s_waitcnt vmcnt(0)\n"
"v_mfma_f32_16x16x32_bf16 v[172:175], v[208:211], v[112:115], v[160:163]\n"
"v_mfma_f32_16x16x32_bf16 v[172:175], v[212:215], v[124:127], v[172:175]\n"
"v_mfma_f32_16x16x32_bf16 v[172:175], v[216:219], v[136:139], v[172:175]\n"
"v_mfma_f32_16x16x32_bf16 v[172:175], v[220:223], v[148:151], v[172:175]\n"
"v_mfma_f32_16x16x32_bf16 v[176:179], v[208:211], v[116:119], v[164:167]\n"
"v_mfma_f32_16x16x32_bf16 v[176:179], v[212:215], v[128:131], v[176:179]\n"
"v_mfma_f32_16x16x32_bf16 v[176:179], v[216:219], v[140:143], v[176:179]\n"
"v_mfma_f32_16x16x32_bf16 v[176:179], v[220:223], v[152:155], v[176:179]\n"
"v_mfma_f32_16x16x32_bf16 v[180:183], v[208:211], v[120:123], v[168:171]\n"
"v_mfma_f32_16x16x32_bf16 v[180:183], v[212:215], v[132:135], v[180:183]\n"
"v_mfma_f32_16x16x32_bf16 v[180:183], v[216:219], v[144:147], v[180:183]\n"
"v_mfma_f32_16x16x32_bf16 v[180:183], v[220:223], v[156:159], v[180:183]\n"
"v_mfma_f32_16x16x32_bf16 v[184:187], v[224:227], v[112:115], v[160:163]\n"
"v_mfma_f32_16x16x32_bf16 v[184:187], v[228:231], v[124:127], v[184:187]\n"
"v_mfma_f32_16x16x32_bf16 v[184:187], v[232:235], v[136:139], v[184:187]\n"
"v_mfma_f32_16x16x32_bf16 v[184:187], v[236:239], v[148:151], v[184:187]\n"
"v_mfma_f32_16x16x32_bf16 v[188:191], v[224:227], v[116:119], v[164:167]\n"
"v_mfma_f32_16x16x32_bf16 v[188:191], v[228:231], v[128:131], v[188:191]\n"
"v_mfma_f32_16x16x32_bf16 v[188:191], v[232:235], v[140:143], v[188:191]\n"
"v_mfma_f32_16x16x32_bf16 v[188:191], v[236:239], v[152:155], v[188:191]\n"
"v_mfma_f32_16x16x32_bf16 v[192:195], v[224:227], v[120:123], v[168:171]\n"
"v_mfma_f32_16x16x32_bf16 v[192:195], v[228:231], v[132:135], v[192:195]\n"
"v_mfma_f32_16x16x32_bf16 v[192:195], v[232:235], v[144:147], v[192:195]\n"
"v_mfma_f32_16x16x32_bf16 v[192:195], v[236:239], v[156:159], v[192:195]\n"
"s_nop 9\n"
"v_exp_f32 v172, v172\n"
"v_exp_f32 v173, v173\n"
"v_exp_f32 v174, v174\n"
"v_exp_f32 v175, v175\n"
"v_exp_f32 v176, v176\n"
"v_exp_f32 v177, v177\n"
"v_exp_f32 v178, v178\n"
"v_exp_f32 v179, v179\n"
"v_exp_f32 v180, v180\n"
"v_exp_f32 v181, v181\n"
"v_exp_f32 v182, v182\n"
"v_exp_f32 v183, v183\n"
"v_exp_f32 v184, v184\n"
"v_exp_f32 v185, v185\n"
"v_exp_f32 v186, v186\n"
"v_exp_f32 v187, v187\n"
"v_exp_f32 v188, v188\n"
"v_exp_f32 v189, v189\n"
"v_exp_f32 v190, v190\n"
"v_exp_f32 v191, v191\n"
"v_exp_f32 v192, v192\n"
"v_exp_f32 v193, v193\n"
"v_exp_f32 v194, v194\n"
"v_exp_f32 v195, v195\n"
"s_nop 1\n"
"v_cvt_pk_bf16_f32 v196, v172, v173\n"
"v_cvt_pk_bf16_f32 v197, v174, v175\n"
"v_cvt_pk_bf16_f32 v198, v184, v185\n"
"v_cvt_pk_bf16_f32 v199, v186, v187\n"
"v_cvt_pk_bf16_f32 v200, v176, v177\n"
"v_cvt_pk_bf16_f32 v201, v178, v179\n"
"v_cvt_pk_bf16_f32 v202, v188, v189\n"
"v_cvt_pk_bf16_f32 v203, v190, v191\n"
"v_cvt_pk_bf16_f32 v204, v180, v181\n"
"v_cvt_pk_bf16_f32 v205, v182, v183\n"
"v_cvt_pk_bf16_f32 v206, v192, v193\n"
"v_cvt_pk_bf16_f32 v207, v194, v195\n"
"s_nop 1\n"
"v_mfma_f32_16x16x32_bf16 v[96:99], v[108:111], v[196:199], v[96:99]\n"
"v_mfma_f32_16x16x32_bf16 v[100:103], v[108:111], v[200:203], v[100:103]\n"
"v_mfma_f32_16x16x32_bf16 v[104:107], v[108:111], v[204:207], v[104:107]\n"
"buffer_load_ushort v224, v241, s[44:47], s63 offen offset:0\n"
"buffer_load_ushort v225, v241, s[44:47], s63 offen offset:256\n"
"buffer_load_ushort v226, v241, s[44:47], s63 offen offset:512\n"
"buffer_load_ushort v227, v241, s[44:47], s63 offen offset:768\n"
"buffer_load_ushort v228, v241, s[44:47], s64 offen offset:0\n"
"buffer_load_ushort v229, v241, s[44:47], s64 offen offset:256\n"
"buffer_load_ushort v230, v241, s[44:47], s64 offen offset:512\n"
"buffer_load_ushort v231, v241, s[44:47], s64 offen offset:768\n"
"buffer_load_ushort v232, v241, s[44:47], s63 offen offset:32\n"
"buffer_load_ushort v233, v241, s[44:47], s63 offen offset:288\n"
"buffer_load_ushort v234, v241, s[44:47], s63 offen offset:544\n"
"buffer_load_ushort v235, v241, s[44:47], s63 offen offset:800\n"
"buffer_load_ushort v236, v241, s[44:47], s64 offen offset:32\n"
"buffer_load_ushort v237, v241, s[44:47], s64 offen offset:288\n"
"buffer_load_ushort v238, v241, s[44:47], s64 offen offset:544\n"
"buffer_load_ushort v239, v241, s[44:47], s64 offen offset:800\n"
"s_waitcnt vmcnt(0)\n"
"v_lshl_or_b32 v208, v225, 16, v224\n"
"v_lshl_or_b32 v209, v227, 16, v226\n"
"v_lshl_or_b32 v210, v229, 16, v228\n"
"v_lshl_or_b32 v211, v231, 16, v230\n"
"v_lshl_or_b32 v212, v233, 16, v232\n"
"v_lshl_or_b32 v213, v235, 16, v234\n"
"v_lshl_or_b32 v214, v237, 16, v236\n"
"v_lshl_or_b32 v215, v239, 16, v238\n"
"s_nop 1\n"
"v_mfma_f32_16x16x32_bf16 v[0:3], v[208:211], v[196:199], v[0:3]\n"
"v_mfma_f32_16x16x32_bf16 v[4:7], v[208:211], v[200:203], v[4:7]\n"
"v_mfma_f32_16x16x32_bf16 v[8:11], v[208:211], v[204:207], v[8:11]\n"
"v_mfma_f32_16x16x32_bf16 v[12:15], v[212:215], v[196:199], v[12:15]\n"
"v_mfma_f32_16x16x32_bf16 v[16:19], v[212:215], v[200:203], v[16:19]\n"
"v_mfma_f32_16x16x32_bf16 v[20:23], v[212:215], v[204:207], v[20:23]\n"
"buffer_load_ushort v224, v241, s[44:47], s63 offen offset:64\n"
"buffer_load_ushort v225, v241, s[44:47], s63 offen offset:320\n"
"buffer_load_ushort v226, v241, s[44:47], s63 offen offset:576\n"
"buffer_load_ushort v227, v241, s[44:47], s63 offen offset:832\n"
"buffer_load_ushort v228, v241, s[44:47], s64 offen offset:64\n"
"buffer_load_ushort v229, v241, s[44:47], s64 offen offset:320\n"
"buffer_load_ushort v230, v241, s[44:47], s64 offen offset:576\n"
"buffer_load_ushort v231, v241, s[44:47], s64 offen offset:832\n"
"buffer_load_ushort v232, v241, s[44:47], s63 offen offset:96\n"
"buffer_load_ushort v233, v241, s[44:47], s63 offen offset:352\n"
"buffer_load_ushort v234, v241, s[44:47], s63 offen offset:608\n"
"buffer_load_ushort v235, v241, s[44:47], s63 offen offset:864\n"
"buffer_load_ushort v236, v241, s[44:47], s64 offen offset:96\n"
"buffer_load_ushort v237, v241, s[44:47], s64 offen offset:352\n"
"buffer_load_ushort v238, v241, s[44:47], s64 offen offset:608\n"
"buffer_load_ushort v239, v241, s[44:47], s64 offen offset:864\n"
"s_waitcnt vmcnt(0)\n"
"v_lshl_or_b32 v208, v225, 16, v224\n"
"v_lshl_or_b32 v209, v227, 16, v226\n"
"v_lshl_or_b32 v210, v229, 16, v228\n"
"v_lshl_or_b32 v211, v231, 16, v230\n"
"v_lshl_or_b32 v212, v233, 16, v232\n"
"v_lshl_or_b32 v213, v235, 16, v234\n"
"v_lshl_or_b32 v214, v237, 16, v236\n"
"v_lshl_or_b32 v215, v239, 16, v238\n"
"s_nop 1\n"
"v_mfma_f32_16x16x32_bf16 v[24:27], v[208:211], v[196:199], v[24:27]\n"
"v_mfma_f32_16x16x32_bf16 v[28:31], v[208:211], v[200:203], v[28:31]\n"
"v_mfma_f32_16x16x32_bf16 v[32:35], v[208:211], v[204:207], v[32:35]\n"
"v_mfma_f32_16x16x32_bf16 v[36:39], v[212:215], v[196:199], v[36:39]\n"
"v_mfma_f32_16x16x32_bf16 v[40:43], v[212:215], v[200:203], v[40:43]\n"
"v_mfma_f32_16x16x32_bf16 v[44:47], v[212:215], v[204:207], v[44:47]\n"
"buffer_load_ushort v224, v241, s[44:47], s63 offen offset:128\n"
"buffer_load_ushort v225, v241, s[44:47], s63 offen offset:384\n"
"buffer_load_ushort v226, v241, s[44:47], s63 offen offset:640\n"
"buffer_load_ushort v227, v241, s[44:47], s63 offen offset:896\n"
"buffer_load_ushort v228, v241, s[44:47], s64 offen offset:128\n"
"buffer_load_ushort v229, v241, s[44:47], s64 offen offset:384\n"
"buffer_load_ushort v230, v241, s[44:47], s64 offen offset:640\n"
"buffer_load_ushort v231, v241, s[44:47], s64 offen offset:896\n"
"buffer_load_ushort v232, v241, s[44:47], s63 offen offset:160\n"
"buffer_load_ushort v233, v241, s[44:47], s63 offen offset:416\n"
"buffer_load_ushort v234, v241, s[44:47], s63 offen offset:672\n"
"buffer_load_ushort v235, v241, s[44:47], s63 offen offset:928\n"
"buffer_load_ushort v236, v241, s[44:47], s64 offen offset:160\n"
"buffer_load_ushort v237, v241, s[44:47], s64 offen offset:416\n"
"buffer_load_ushort v238, v241, s[44:47], s64 offen offset:672\n"
"buffer_load_ushort v239, v241, s[44:47], s64 offen offset:928\n"
"s_waitcnt vmcnt(0)\n"
"v_lshl_or_b32 v208, v225, 16, v224\n"
"v_lshl_or_b32 v209, v227, 16, v226\n"
"v_lshl_or_b32 v210, v229, 16, v228\n"
"v_lshl_or_b32 v211, v231, 16, v230\n"
"v_lshl_or_b32 v212, v233, 16, v232\n"
"v_lshl_or_b32 v213, v235, 16, v234\n"
"v_lshl_or_b32 v214, v237, 16, v236\n"
"v_lshl_or_b32 v215, v239, 16, v238\n"
"s_nop 1\n"
"v_mfma_f32_16x16x32_bf16 v[48:51], v[208:211], v[196:199], v[48:51]\n"
"v_mfma_f32_16x16x32_bf16 v[52:55], v[208:211], v[200:203], v[52:55]\n"
"v_mfma_f32_16x16x32_bf16 v[56:59], v[208:211], v[204:207], v[56:59]\n"
"v_mfma_f32_16x16x32_bf16 v[60:63], v[212:215], v[196:199], v[60:63]\n"
"v_mfma_f32_16x16x32_bf16 v[64:67], v[212:215], v[200:203], v[64:67]\n"
"v_mfma_f32_16x16x32_bf16 v[68:71], v[212:215], v[204:207], v[68:71]\n"
"buffer_load_ushort v224, v241, s[44:47], s63 offen offset:192\n"
"buffer_load_ushort v225, v241, s[44:47], s63 offen offset:448\n"
"buffer_load_ushort v226, v241, s[44:47], s63 offen offset:704\n"
"buffer_load_ushort v227, v241, s[44:47], s63 offen offset:960\n"
"buffer_load_ushort v228, v241, s[44:47], s64 offen offset:192\n"
"buffer_load_ushort v229, v241, s[44:47], s64 offen offset:448\n"
"buffer_load_ushort v230, v241, s[44:47], s64 offen offset:704\n"
"buffer_load_ushort v231, v241, s[44:47], s64 offen offset:960\n"
"buffer_load_ushort v232, v241, s[44:47], s63 offen offset:224\n"
"buffer_load_ushort v233, v241, s[44:47], s63 offen offset:480\n"
"buffer_load_ushort v234, v241, s[44:47], s63 offen offset:736\n"
"buffer_load_ushort v235, v241, s[44:47], s63 offen offset:992\n"
"buffer_load_ushort v236, v241, s[44:47], s64 offen offset:224\n"
"buffer_load_ushort v237, v241, s[44:47], s64 offen offset:480\n"
"buffer_load_ushort v238, v241, s[44:47], s64 offen offset:736\n"
"buffer_load_ushort v239, v241, s[44:47], s64 offen offset:992\n"
"s_waitcnt vmcnt(0)\n"
"v_lshl_or_b32 v208, v225, 16, v224\n"
"v_lshl_or_b32 v209, v227, 16, v226\n"
"v_lshl_or_b32 v210, v229, 16, v228\n"
"v_lshl_or_b32 v211, v231, 16, v230\n"
"v_lshl_or_b32 v212, v233, 16, v232\n"
"v_lshl_or_b32 v213, v235, 16, v234\n"
"v_lshl_or_b32 v214, v237, 16, v236\n"
"v_lshl_or_b32 v215, v239, 16, v238\n"
"s_nop 1\n"
"v_mfma_f32_16x16x32_bf16 v[72:75], v[208:211], v[196:199], v[72:75]\n"
"v_mfma_f32_16x16x32_bf16 v[76:79], v[208:211], v[200:203], v[76:79]\n"
"v_mfma_f32_16x16x32_bf16 v[80:83], v[208:211], v[204:207], v[80:83]\n"
"v_mfma_f32_16x16x32_bf16 v[84:87], v[212:215], v[196:199], v[84:87]\n"
"v_mfma_f32_16x16x32_bf16 v[88:91], v[212:215], v[200:203], v[88:91]\n"
"v_mfma_f32_16x16x32_bf16 v[92:95], v[212:215], v[204:207], v[92:95]\n"
"s_nop 9\n"
"s_add_u32 s63, s63, 8192\n"
"s_cmp_lt_u32 s63, 131072\n"
"s_cbranch_scc1 .Lg_slatt_%=\n"
"v_rcp_f32 v172, v96\n"
"v_rcp_f32 v174, v100\n"
"v_rcp_f32 v176, v104\n"
"s_nop 1\n"
"v_mul_f32 v0, v172, v0\n"
"v_mul_f32 v1, v172, v1\n"
"v_mul_f32 v2, v172, v2\n"
"v_mul_f32 v3, v172, v3\n"
"v_mul_f32 v12, v172, v12\n"
"v_mul_f32 v13, v172, v13\n"
"v_mul_f32 v14, v172, v14\n"
"v_mul_f32 v15, v172, v15\n"
"v_cvt_pk_bf16_f32 v160, v0, v1\n"
"v_cvt_pk_bf16_f32 v161, v2, v3\n"
"v_cvt_pk_bf16_f32 v162, v12, v13\n"
"v_cvt_pk_bf16_f32 v163, v14, v15\n"
"ds_write_b128 v254, v[160:163] offset:0\n"
"v_mul_f32 v24, v172, v24\n"
"v_mul_f32 v25, v172, v25\n"
"v_mul_f32 v26, v172, v26\n"
"v_mul_f32 v27, v172, v27\n"
"v_mul_f32 v36, v172, v36\n"
"v_mul_f32 v37, v172, v37\n"
"v_mul_f32 v38, v172, v38\n"
"v_mul_f32 v39, v172, v39\n"
"v_cvt_pk_bf16_f32 v164, v24, v25\n"
"v_cvt_pk_bf16_f32 v165, v26, v27\n"
"v_cvt_pk_bf16_f32 v166, v36, v37\n"
"v_cvt_pk_bf16_f32 v167, v38, v39\n"
"ds_write_b128 v254, v[164:167] offset:3072\n"
"v_mul_f32 v48, v172, v48\n"
"v_mul_f32 v49, v172, v49\n"
"v_mul_f32 v50, v172, v50\n"
"v_mul_f32 v51, v172, v51\n"
"v_mul_f32 v60, v172, v60\n"
"v_mul_f32 v61, v172, v61\n"
"v_mul_f32 v62, v172, v62\n"
"v_mul_f32 v63, v172, v63\n"
"v_cvt_pk_bf16_f32 v168, v48, v49\n"
"v_cvt_pk_bf16_f32 v169, v50, v51\n"
"v_cvt_pk_bf16_f32 v170, v60, v61\n"
"v_cvt_pk_bf16_f32 v171, v62, v63\n"
"ds_write_b128 v254, v[168:171] offset:6144\n"
"s_branch .Lg_slowret_%=\n"
      :
      : "s"(__builtin_amdgcn_kernarg_segment_ptr()), "s"(blockIdx.x), "s"(wid), "s"((u32)(size_t)lds)
      : "memory", "vcc", "scc",
        "v0","v1","v2","v3","v4","v5","v6","v7","v8","v9", VC8(1), VC8(2), VC8(3), VC8(4), VC8(5), VC8(6), VC8(7), VC8(8), VC8(9),
        VC8(10), VC8(11), VC8(12), VC8(13), VC8(14), VC8(15), VC8(16), VC8(17), VC8(18), VC8(19), VC8(20), VC8(21), VC8(22), VC8(23), VC8(24),
        "v250","v251","v252","v253","v254","v255",
        "s6","s7","s8","s9","s10","s11","s12","s13","s14","s15","s16","s17","s18","s19", SC8(2), "s30","s31","s33","s34","s35","s36","s37","s38","s39",
        SC8(4), SC8(5), SC8(6), SC8(7), SC8(8), SC8(9));
}

extern "C" void kernel_launch(void* const* d_in, const int* in_sizes, int n_in, void* d_out, int out_size, void* d_ws, size_t ws_size,
                              hipStream_t stream) {
  gqa_main<<<256, 512, 0, stream>>>(d_in[0], d_in[1], d_in[2], d_in[3], d_in[4], d_out, d_ws, d_ws);
}
```
